```python
import torch

def _softmax(x, axis=-1):
    return torch.softmax(x, dim=axis)
def _gelu(x):
    return torch.nn.functional.gelu(x, approximate="tanh")
def _silu(x):
    return torch.nn.functional.silu(x)
def _sigmoid(x):
    return torch.sigmoid(x)
def _layernorm(x, axis=-1, eps=1e-5):
    m = x.mean(dim=axis, keepdim=True)
    v = x.var(dim=axis, keepdim=True, unbiased=False)
    return (x - m) / torch.sqrt(v + eps)
def _scatter_add(tgt, idx, src, axis=0):
    return tgt.clone().index_add_(axis, idx, src)
def _segment_sum(src, seg, n):
    out = torch.zeros((n,) + src.shape[1:], dtype=src.dtype)
    return out.index_add_(0, seg, src)
def _pad(x, pw):
    flat = tuple(v for pair in reversed(pw) for v in pair)
    return torch.nn.functional.pad(x, flat)

kernel_name = 'recurrent_scan__gqa_block_cached__glu_gated_geglu__decode'

def setup_inputs():

    _w = (lambda a: (lambda t: t.to(torch.bfloat16) if t.is_floating_point() else t)(torch.as_tensor(a)))
    def _A_setup():
        B, Sq, D = 4, 1, 1024
        Hq, Hkv, Sk, Dh = 32, 4, 8192, 128
        assert Hq % Hkv == 0, (Hq, Hkv)
        return {
            "x": _w((lambda *s: torch.randn(*s, dtype=torch.bfloat16) * 0.4)(B, Sq, D)),
            "k_cache": _w((lambda *s: torch.randn(*s, dtype=torch.bfloat16) * 1.6)(B, Hkv, Sk, Dh)),
            "v_cache": _w((lambda *s: torch.randn(*s, dtype=torch.bfloat16) * 1.6)(B, Hkv, Sk, Dh)),
            "wq": _w((lambda *s: torch.randn(*s, dtype=torch.bfloat16) * 1.6)(D, Hq * Dh) * (D ** -0.5)),
            "wo": _w((lambda *s: torch.randn(*s, dtype=torch.bfloat16) * 1.6)(Hq * Dh, D) * ((Hq * Dh) ** -0.5)),
        }
    _A_in = _A_setup()

    def _B_setup():
        B, S, Dm, Df = 4, 1, 1024, 3584
        return {
            "x": _w((lambda *s: torch.randn(*s, dtype=torch.bfloat16) * 0.4)(B, S, Dm)),
            "w_gate": _w((lambda *s: torch.randn(*s, dtype=torch.bfloat16) * 1.6)(Dm, Df) * (Dm ** -0.5)),
            "w_up": _w((lambda *s: torch.randn(*s, dtype=torch.bfloat16) * 1.6)(Dm, Df) * (Dm ** -0.5)),
            "w_down": _w((lambda *s: torch.randn(*s, dtype=torch.bfloat16) * 1.6)(Df, Dm) * (Df ** -0.5)),
        }
    _B_in = _B_setup()

    return {
        "A_x": _A_in["x"],
        "A_k_cache": _A_in["k_cache"],
        "A_v_cache": _A_in["v_cache"],
        "A_wq": _A_in["wq"],
        "A_wo": _A_in["wo"],
        "B_x": _B_in["x"],
        "B_w_gate": _B_in["w_gate"],
        "B_w_up": _B_in["w_up"],
        "B_w_down": _B_in["w_down"],
    }

def reference(A_x, A_k_cache, A_v_cache, A_wq, A_wo, B_x, B_w_gate, B_w_up, B_w_down):
    def _A_ref(x, k_cache, v_cache, wq, wo):
        B, Sq, D = x.shape
        Hq, Dh = 32, 128
        Hkv = k_cache.shape[1]
        rep = Hq // Hkv
        q = (lambda x, *d: x.permute(*d))((x @ wq).reshape(B, Sq, Hq, Dh), 0, 2, 1, 3)
        k = (lambda x, n, axis: torch.repeat_interleave(x, n, dim=axis))(k_cache, rep, axis=1)
        v = (lambda x, n, axis: torch.repeat_interleave(x, n, dim=axis))(v_cache, rep, axis=1)
        scores = torch.einsum("bhqd,bhkd->bhqk", q, k) * (Dh ** -0.5)
        p = _softmax(scores, axis=-1)
        o = torch.einsum("bhqk,bhkd->bhqd", p, v)
        return (lambda x, *d: x.permute(*d))(o, 0, 2, 1, 3).reshape(B, Sq, Hq * Dh) @ wo

    def _B_ref(x, w_gate, w_up, w_down):
        g = torch.einsum("bsd,df->bsf", x, w_gate)
        u = torch.einsum("bsd,df->bsf", x, w_up)
        h = _gelu(g) * u
        return torch.einsum("bsf,fd->bsd", h, w_down)

    x = A_x
    xn = _layernorm(x, axis=-1)
    s = _A_ref(xn, A_k_cache, A_v_cache, A_wq, A_wo)
    g = _silu(_B_ref(xn, B_w_gate, B_w_up, B_w_down))
    return x + s * g
```

```cpp
#include <hip/hip_runtime.h>
#include <hip/hip_bf16.h>
#include <stdint.h>

#define MIMIC 0

typedef unsigned short u16;
__device__ __forceinline__ float bf2f(u16 v) { return __uint_as_float(((unsigned)v) << 16); }
__device__ __forceinline__ u16 f2bf(float f) {
  unsigned u = __float_as_uint(f);
  if ((u & 0x7fffffffu) > 0x7f800000u) return (u16)((u >> 16) | 0x40);
  return (u16)((u + 0x7fffu + ((u >> 16) & 1u)) >> 16);
}
__device__ __forceinline__ float rbf(float f) { return bf2f(f2bf(f)); }

constexpr int B = 4, D = 1024, HQ = 32, HKV = 4, SK = 8192, DH = 128, DF = 3584;
constexpr int NSPLIT = 16, KPS = SK / NSPLIT;

__global__ void k_ln(const u16* x, u16* xn) {
  int b = blockIdx.x, t = threadIdx.x;
  __shared__ float red[256];
  float v[4]; float s = 0;
  for (int i = 0; i < 4; ++i) { v[i] = bf2f(x[b * D + t * 4 + i]); s += v[i]; }
  red[t] = s; __syncthreads();
  for (int o = 128; o > 0; o >>= 1) { if (t < o) red[t] += red[t + o]; __syncthreads(); }
  float mean = red[0] / D; __syncthreads();
  float q = 0;
  for (int i = 0; i < 4; ++i) { float d = v[i] - mean; q += d * d; }
  red[t] = q; __syncthreads();
  for (int o = 128; o > 0; o >>= 1) { if (t < o) red[t] += red[t + o]; __syncthreads(); }
  float var = red[0] / D;
  float mb = rbf(mean), vb = rbf(var);
  float den = rbf(sqrtf(rbf(vb + 1e-5f)));
  for (int i = 0; i < 4; ++i) xn[b * D + t * 4 + i] = f2bf(rbf(v[i] - mb) / den);
}

__device__ __forceinline__ float gelu_tanh(float x) {
  const float k0 = 0.7978845608028654f, k1 = 0.044715f;
  float inner = k0 * (x + k1 * x * x * x);
  return 0.5f * x * (1.f + tanhf(inner));
}

__global__ void k_proj1(const u16* xn, const u16* wq, const u16* wg, const u16* wu, u16* q, u16* h) {
  __shared__ float xs[B][D];
  __shared__ float red[2][4][64][B];
  int t = threadIdx.x;
  for (int i = t; i < B * D; i += 256) xs[i / D][i % D] = bf2f(xn[i]);
  __syncthreads();
  int c = t & 63, kg = t >> 6;
  int blk = blockIdx.x;
  if (blk < 64) {
    int col = blk * 64 + c;
    float a[B] = {0, 0, 0, 0};
    for (int k = kg * 256; k < kg * 256 + 256; ++k) {
      float w = bf2f(wq[(size_t)k * 4096 + col]);
      for (int b = 0; b < B; ++b) a[b] = fmaf(xs[b][k], w, a[b]);
    }
    for (int b = 0; b < B; ++b) red[0][kg][c][b] = a[b];
    __syncthreads();
    if (kg == 0) for (int b = 0; b < B; ++b) q[b * 4096 + col] = f2bf(red[0][0][c][b] + red[0][1][c][b] + red[0][2][c][b] + red[0][3][c][b]);
  } else {
    int col = (blk - 64) * 64 + c;
    float a[B] = {0, 0, 0, 0}, u[B] = {0, 0, 0, 0};
    for (int k = kg * 256; k < kg * 256 + 256; ++k) {
      float w = bf2f(wg[(size_t)k * DF + col]), w2 = bf2f(wu[(size_t)k * DF + col]);
      for (int b = 0; b < B; ++b) { a[b] = fmaf(xs[b][k], w, a[b]); u[b] = fmaf(xs[b][k], w2, u[b]); }
    }
    for (int b = 0; b < B; ++b) { red[0][kg][c][b] = a[b]; red[1][kg][c][b] = u[b]; }
    __syncthreads();
    if (kg == 0) for (int b = 0; b < B; ++b) {
      float g = rbf(red[0][0][c][b] + red[0][1][c][b] + red[0][2][c][b] + red[0][3][c][b]);
      float uu = rbf(red[1][0][c][b] + red[1][1][c][b] + red[1][2][c][b] + red[1][3][c][b]);
      h[b * DF + col] = f2bf(rbf(gelu_tanh(g)) * uu);
    }
  }
}

__global__ void k_attn(const u16* q, const u16* kc, const u16* vc, float* pm, float* pl, float* po) {
  int s = blockIdx.x, kvh = blockIdx.y, b = blockIdx.z, t = threadIdx.x;
  __shared__ float qs[8][DH];
  __shared__ float sc[8][KPS];
  __shared__ float mx[8], sm[8];
  for (int i = t; i < 8 * DH; i += 256) qs[i / DH][i % DH] = bf2f(q[b * 4096 + kvh * 1024 + i]);
  __syncthreads();
  const u16* kb = kc + ((size_t)(b * HKV + kvh) * SK + s * KPS) * DH;
  const u16* vb = vc + ((size_t)(b * HKV + kvh) * SK + s * KPS) * DH;
  for (int key = t; key < KPS; key += 256) {
    float a[8] = {0, 0, 0, 0, 0, 0, 0, 0};
    for (int d = 0; d < DH; ++d) { float kv = bf2f(kb[(size_t)key * DH + d]); for (int hh = 0; hh < 8; ++hh) a[hh] = fmaf(qs[hh][d], kv, a[hh]); }
    for (int hh = 0; hh < 8; ++hh) {
      sc[hh][key] = a[hh] * 0.08838834764831845f;
    }
  }
  __syncthreads();
  if (t < 8) {
    float m = -INFINITY; for (int k = 0; k < KPS; ++k) m = fmaxf(m, sc[t][k]);
    float l = 0; for (int k = 0; k < KPS; ++k) { float p = __expf(sc[t][k] - m); sc[t][k] = p; l += p; }
    mx[t] = m; sm[t] = l;
  }
  __syncthreads();
  int d = t & 127, hg = t >> 7;
  float o[4] = {0, 0, 0, 0};
  for (int k = 0; k < KPS; ++k) { float v = bf2f(vb[(size_t)k * DH + d]); for (int i = 0; i < 4; ++i) o[i] = fmaf(sc[hg * 4 + i][k], v, o[i]); }
  for (int i = 0; i < 4; ++i) { int head = kvh * 8 + hg * 4 + i; po[((size_t)(b * HQ + head) * NSPLIT + s) * DH + d] = o[i]; }
  if (t < 8) { int head = kvh * 8 + t; pm[(b * HQ + head) * NSPLIT + s] = mx[t]; pl[(b * HQ + head) * NSPLIT + s] = sm[t]; }
}

__global__ void k_combine(const float* pm, const float* pl, const float* po, u16* o) {
  int row = blockIdx.x, d = threadIdx.x;
  float M = -INFINITY; for (int s = 0; s < NSPLIT; ++s) M = fmaxf(M, pm[row * NSPLIT + s]);
  float L = 0, a = 0;
  for (int s = 0; s < NSPLIT; ++s) { float w = __expf(pm[row * NSPLIT + s] - M); L += w * pl[row * NSPLIT + s]; a += w * po[((size_t)row * NSPLIT + s) * DH + d]; }
  o[row * DH + d] = f2bf(a / L);
}

__global__ void k_out(const u16* x, const u16* o, const u16* h, const u16* wo, const u16* wd, u16* out) {
  __shared__ float red[2][16][64][B];
  int t = threadIdx.x, c = t & 63, kg = t >> 6, col = blockIdx.x * 64 + c;
  float a[B] = {0, 0, 0, 0}, u[B] = {0, 0, 0, 0};
  for (int k = kg * 256; k < kg * 256 + 256; ++k) { float w = bf2f(wo[(size_t)k * D + col]); for (int b = 0; b < B; ++b) a[b] = fmaf(bf2f(o[b * 4096 + k]), w, a[b]); }
  for (int k = kg * 224; k < kg * 224 + 224; ++k) { float w = bf2f(wd[(size_t)k * D + col]); for (int b = 0; b < B; ++b) u[b] = fmaf(bf2f(h[b * DF + k]), w, u[b]); }
  for (int b = 0; b < B; ++b) { red[0][kg][c][b] = a[b]; red[1][kg][c][b] = u[b]; }
  __syncthreads();
  if (kg < B) {
    int b = kg; float s = 0, d = 0;
    for (int i = 0; i < 16; ++i) { s += red[0][i][c][b]; d += red[1][i][c][b]; }
    s = rbf(s); d = rbf(d);
    float g = rbf(d / (1.f + __expf(-d)));
    out[b * D + col] = f2bf(bf2f(x[b * D + col]) + rbf(s * g));
  }
}

extern "C" void kernel_launch(void* const* d_in, const int* in_sizes, int n_in, void* d_out, int out_size, void* d_ws, size_t ws_size, hipStream_t stream) {
  const u16* x = (const u16*)d_in[0]; const u16* kc = (const u16*)d_in[1]; const u16* vc = (const u16*)d_in[2];
  const u16* wq = (const u16*)d_in[3]; const u16* wo = (const u16*)d_in[4];
  const u16* wg = (const u16*)d_in[6]; const u16* wu = (const u16*)d_in[7]; const u16* wd = (const u16*)d_in[8];
  char* ws = (char*)d_ws;
  u16* xn = (u16*)ws; u16* q = (u16*)(ws + 65536); u16* h = (u16*)(ws + 131072); u16* o = (u16*)(ws + 196608);
  float* pm = (float*)(ws + 262144); float* pl = (float*)(ws + 327680); float* po = (float*)(ws + 1048576);
  k_ln<<<B, 256, 0, stream>>>(x, xn);
  k_proj1<<<64 + 56, 256, 0, stream>>>(xn, wq, wg, wu, q, h);
  k_attn<<<dim3(NSPLIT, HKV, B), 256, 0, stream>>>(q, kc, vc, pm, pl, po);
  k_combine<<<B * HQ, 128, 0, stream>>>(pm, pl, po, o);
  k_out<<<16, 1024, 0, stream>>>(x, o, h, wo, wd, (u16*)d_out);
}
```

```cpp
#include <hip/hip_runtime.h>
#include <stdint.h>

typedef unsigned u32;
typedef unsigned long long u64;
typedef u32 u32x4 __attribute__((ext_vector_type(4)));
typedef u32 u32x2 __attribute__((ext_vector_type(2)));
typedef float f32x4 __attribute__((ext_vector_type(4)));
typedef __bf16 bf16x8 __attribute__((ext_vector_type(8)));
typedef __amdgpu_buffer_rsrc_t rsrc_t;

constexpr int NB = 4, DM = 1024, HQ = 32, HKV = 4, SK = 8192, DH = 128, DF = 3584;
constexpr int NWG = 256, NTHR = 256;

constexpr u32 WS_E = 0;
constexpr u32 WS_QF = 4096;
constexpr u32 WS_QP = 8192;
constexpr u32 WS_GU = WS_QP + 8 * 4 * 4096 * 4;
constexpr u32 WS_OP = WS_GU + 8 * 2 * DF * 16;
constexpr u32 WS_ML = WS_OP + 4 * 32 * 16 * 512;
constexpr u32 WS_FP = WS_ML + 4 * 32 * 16 * 16;
constexpr u32 WS_END = WS_FP + 60 * 1024 * 16;

constexpr int L_K = 0;
constexpr int L_REDQ = 131072;
constexpr int L_REDGU = L_REDQ + 8192;
constexpr int L_XN = L_REDGU + 16384;
constexpr int L_QS = L_XN + 1024;
constexpr int L_HS = L_QS + 2048;
constexpr int L_OS = L_HS + 1024;
constexpr int L_ML = L_OS + 1024;
constexpr int L_END = L_ML + 256;
constexpr int LK_MRG = 0;
constexpr int LK_RD = 8192;
constexpr int LK_RO = 12288;
constexpr int LK_FIN = 16384;

struct Params {
  const char* x; const char* k; const char* v; const char* wq; const char* wo;
  const char* wg; const char* wu; const char* wd; char* out; char* ws;
};

#define SB() __builtin_amdgcn_sched_barrier(0)
#define KEEP(x) do { SB(); asm volatile("" ::"v"(x)); } while (0)

__device__ __forceinline__ rsrc_t mk(const char* p, u32 bytes) {
  return __builtin_amdgcn_make_buffer_rsrc((void*)p, 0, bytes, 0x00020000);
}
__device__ __forceinline__ u32x4 ld_nt(rsrc_t r, u32 voff, u32 soff) { return __builtin_amdgcn_raw_buffer_load_b128(r, voff, soff, 2); }
__device__ __forceinline__ u32x4 ld_pl(rsrc_t r, u32 voff, u32 soff) { return __builtin_amdgcn_raw_buffer_load_b128(r, voff, soff, 0); }
__device__ __forceinline__ u32x4 ld_sc(rsrc_t r, u32 voff, u32 soff) { return __builtin_amdgcn_raw_buffer_load_b128(r, voff, soff, 16); }
__device__ __forceinline__ u32x2 ld_sc64(rsrc_t r, u32 voff, u32 soff) { return __builtin_amdgcn_raw_buffer_load_b64(r, voff, soff, 16); }
__device__ __forceinline__ void st_sc(u32x4 v, rsrc_t r, u32 voff, u32 soff) { __builtin_amdgcn_raw_buffer_store_b128(v, r, voff, soff, 16); }
__device__ __forceinline__ void st_sc64(u32x2 v, rsrc_t r, u32 voff, u32 soff) { __builtin_amdgcn_raw_buffer_store_b64(v, r, voff, soff, 16); }

__device__ __forceinline__ void wg_barrier() {
  asm volatile("s_waitcnt lgkmcnt(0)\n\ts_barrier" ::: "memory");
}
__device__ __forceinline__ float bf_lo(u32 v) { return __uint_as_float(v << 16); }
__device__ __forceinline__ float bf_hi(u32 v) { return __uint_as_float(v & 0xffff0000u); }
__device__ __forceinline__ u32 cvt_pk(float lo, float hi) {
  u32 r; asm("v_cvt_pk_bf16_f32 %0, %1, %2" : "=v"(r) : "v"(lo), "v"(hi)); return r;
}
__device__ __forceinline__ float rbf(float f) { return bf_lo(cvt_pk(f, f)); }

template <int CTRL> __device__ __forceinline__ float dpp(float x) {
  return __builtin_bit_cast(float, __builtin_amdgcn_mov_dpp(__builtin_bit_cast(int, x), CTRL, 0xf, 0xf, true));
}
constexpr int XOR1 = 0xB1, XOR2 = 0x4E, XOR7 = 0x141, XOR8 = 0x128;
__device__ __forceinline__ float xrow_sum(float x) {
  auto s = __builtin_amdgcn_permlane16_swap(__float_as_uint(x), __float_as_uint(x), false, false);
  x = __uint_as_float(s[0]) + __uint_as_float(s[1]);
  auto t = __builtin_amdgcn_permlane32_swap(__float_as_uint(x), __float_as_uint(x), false, false);
  return __uint_as_float(t[0]) + __uint_as_float(t[1]);
}
__device__ __forceinline__ float xrow_max(float x) {
  auto s = __builtin_amdgcn_permlane16_swap(__float_as_uint(x), __float_as_uint(x), false, false);
  x = fmaxf(__uint_as_float(s[0]), __uint_as_float(s[1]));
  auto t = __builtin_amdgcn_permlane32_swap(__float_as_uint(x), __float_as_uint(x), false, false);
  return fmaxf(__uint_as_float(t[0]), __uint_as_float(t[1]));
}
__device__ __forceinline__ float x32_sum(float x) {
  auto t = __builtin_amdgcn_permlane32_swap(__float_as_uint(x), __float_as_uint(x), false, false);
  return __uint_as_float(t[0]) + __uint_as_float(t[1]);
}
__device__ __forceinline__ float wave_sum(float x) {
  x += dpp<XOR1>(x); x += dpp<XOR2>(x); x += dpp<XOR7>(x); x += dpp<XOR8>(x);
  return xrow_sum(x);
}
__device__ __forceinline__ float row16_max(float x) {
  x = fmaxf(x, dpp<XOR1>(x)); x = fmaxf(x, dpp<XOR2>(x)); x = fmaxf(x, dpp<XOR7>(x)); x = fmaxf(x, dpp<XOR8>(x));
  return x;
}
__device__ __forceinline__ float row16_sum(float x) {
  x += dpp<XOR1>(x); x += dpp<XOR2>(x); x += dpp<XOR7>(x); x += dpp<XOR8>(x);
  return x;
}
__device__ __forceinline__ float fexp(float x) { return __builtin_amdgcn_exp2f(x * 1.4426950408889634f); }

__device__ __forceinline__ f32x4 mfma(u32x4 a, u32x4 b, f32x4 c) {
  return __builtin_amdgcn_mfma_f32_16x16x32_bf16(__builtin_bit_cast(bf16x8, a), __builtin_bit_cast(bf16x8, b), c, 0, 0, 0);
}
__device__ __forceinline__ u32x4 colfrag(const u32x4 (&w)[8], int i) {
  const u32 sel = (i & 1) ? 0x07060302u : 0x05040100u;
  const int d = i >> 1;
  u32x4 f;
  f[0] = __builtin_amdgcn_perm(w[1][d], w[0][d], sel);
  f[1] = __builtin_amdgcn_perm(w[3][d], w[2][d], sel);
  f[2] = __builtin_amdgcn_perm(w[5][d], w[4][d], sel);
  f[3] = __builtin_amdgcn_perm(w[7][d], w[6][d], sel);
  return f;
}
__device__ __forceinline__ u32 tag1(float v, u32 t) { return ((__float_as_uint(v) + 0x800u) & 0xfffff000u) | t; }
__device__ __forceinline__ u32x4 tag4(f32x4 v, u32 t) { u32x4 r = {tag1(v[0], t), tag1(v[1], t), tag1(v[2], t), tag1(v[3], t)}; return r; }
__device__ __forceinline__ u32 tagbad(u32x4 v, u32 t) { return ((v[0] ^ t) | (v[1] ^ t) | (v[2] ^ t) | (v[3] ^ t)) & 0xfffu; }
__device__ __forceinline__ f32x4 untag(u32x4 v) {
  f32x4 r = {__uint_as_float(v[0] & 0xfffff000u), __uint_as_float(v[1] & 0xfffff000u), __uint_as_float(v[2] & 0xfffff000u), __uint_as_float(v[3] & 0xfffff000u)};
  return r;
}
constexpr int SPIN_CAP = 1 << 18;

#define STAMP(i) do {} while (0)

__global__ void __launch_bounds__(NTHR, 1) fused_decode_kernel(Params p) {
  extern __shared__ __attribute__((aligned(16))) char lds[];
  const int tid = threadIdx.x, lane = tid & 63;
  const int wave = __builtin_amdgcn_readfirstlane(tid >> 6);
  const int c = lane & 15, g = lane >> 4;
  const int bid = blockIdx.x, xcd = bid & 7, r = bid >> 3;
  STAMP(0);

  const int pair = xcd * 2 + (r >> 4), b_att = pair >> 2, kvh = pair & 3, split = r & 15;
  const bool has_ff = r < 28;
  const int td = xcd * 28 + r, rg = td >> 3, cgd = td & 7;
  const int head_o = xcd * 4 + (r >> 3), cgo = r & 7;

  const rsrc_t rs_ws = mk(p.ws, WS_END);
  const rsrc_t rs_wsf = mk(p.ws, has_ff ? WS_END : 0u);
  const rsrc_t rs_x = mk(p.x, NB * DM * 2);
  const rsrc_t rs_wq = mk(p.wq + (size_t)(128 * xcd) * 8192 + r * 256, 128 * 8192 - r * 256);
  const rsrc_t rs_wg = mk(p.wg + (size_t)(128 * xcd) * 7168 + (has_ff ? r : 0) * 256, has_ff ? 128 * 7168 - r * 256 : 0);
  const rsrc_t rs_wu = mk(p.wu + (size_t)(128 * xcd) * 7168 + (has_ff ? r : 0) * 256, has_ff ? 128 * 7168 - r * 256 : 0);
  const rsrc_t rs_k = mk(p.k + ((size_t)pair * SK + split * 512) * 256, 512 * 256);
  const rsrc_t rs_v = mk(p.v + ((size_t)pair * SK + split * 512) * 256, 512 * 256);
  const rsrc_t rs_wd = mk(p.wd + (size_t)(has_ff ? rg : 0) * 128 * 2048 + cgd * 256, has_ff ? 128 * 2048 - cgd * 256 : 0);
  const rsrc_t rs_wo = mk(p.wo + (size_t)head_o * 128 * 2048 + cgo * 256, 128 * 2048 - cgo * 256);

  u32x2 e2 = ld_sc64(rs_ws, WS_E, 0);
  u32x4 x0 = ld_pl(rs_x, wave * 2048 + lane * 32, 0);
  u32x4 x1 = ld_pl(rs_x, wave * 2048 + lane * 32 + 16, 0);
  const u32 xs = __builtin_amdgcn_raw_buffer_load_b32(rs_x, wave * 2048 + xcd * 256 + lane * 4, 0, 0);
  u32 cw0;
  {
    u64 pc; u32 len;
    asm volatile("s_getpc_b64 %0\n.Lfdk_here:\n\ts_mov_b32 %1, .Lfdk_end-.Lfdk_here" : "=s"(pc), "=s"(len));
    len = (int)len > 0 ? len : 0u;
    const rsrc_t rs_c = mk((const char*)pc, len);
    cw0 = __builtin_amdgcn_raw_buffer_load_b32(rs_c, (wave * 64 + lane) * 128, 0, 0);
  }
  SB();
  u32x4 wq[8], wg[8], wu[8];
  const u32 vo8 = (u32)(wave * 32 + g * 8) * 8192 + c * 16;
  const u32 vo7 = (u32)(wave * 32 + g * 8) * 7168 + c * 16;
  const u32 vo2 = (u32)(wave * 32 + g * 8) * 2048 + c * 16;
#pragma unroll
  for (int j = 0; j < 8; ++j) wq[j] = ld_nt(rs_wq, vo8, j * 8192);
#pragma unroll
  for (int j = 0; j < 8; ++j) wg[j] = ld_nt(rs_wg, vo7, j * 7168);
  SB();

  {
    float v[16];
#pragma unroll
    for (int i = 0; i < 4; ++i) { v[2 * i] = bf_lo(x0[i]); v[2 * i + 1] = bf_hi(x0[i]); v[8 + 2 * i] = bf_lo(x1[i]); v[8 + 2 * i + 1] = bf_hi(x1[i]); }
    float s = 0.f;
#pragma unroll
    for (int i = 0; i < 16; ++i) s += v[i];
    const float mean = wave_sum(s) * (1.f / DM);
    float q = 0.f;
#pragma unroll
    for (int i = 0; i < 16; ++i) { const float d = v[i] - mean; q = fmaf(d, d, q); }
    const float var = wave_sum(q) * (1.f / DM);
    const float mb = rbf(mean), vb = rbf(var);
    const float den = rbf(sqrtf(rbf(vb + 1e-5f)));
    const u32 o2 = cvt_pk(rbf(bf_lo(xs) - mb) / den, rbf(bf_hi(xs) - mb) / den);
    *(u32*)(lds + L_XN + wave * 256 + lane * 4) = o2;
  }
  wg_barrier();
  STAMP(1);
  const u32x4 xf = *(const u32x4*)(lds + L_XN + (c & 3) * 256 + wave * 64 + g * 16);
  const u64 epoch = (((u64)(u32)__builtin_amdgcn_readfirstlane(e2[1])) << 32 | (u32)__builtin_amdgcn_readfirstlane(e2[0])) + 1ull;
  const u32 T12 = (u32)epoch & 0xfffu;

  KEEP(wq[7]);
  SB();
#pragma unroll
  for (int j = 0; j < 8; ++j) wu[j] = ld_nt(rs_wu, vo7, j * 7168);
  SB();
  {
    f32x4 acc[8];
#pragma unroll
    for (int i = 0; i < 8; ++i) acc[i] = mfma(xf, colfrag(wq, i), f32x4{0.f, 0.f, 0.f, 0.f});
    if (g == 0) {
      char* d = lds + L_REDQ + wave * 2048 + c * 32;
#pragma unroll
      for (int b = 0; b < 4; ++b) {
        *(f32x4*)(d + b * 512) = f32x4{acc[0][b], acc[1][b], acc[2][b], acc[3][b]};
        *(f32x4*)(d + b * 512 + 16) = f32x4{acc[4][b], acc[5][b], acc[6][b], acc[7][b]};
      }
    }
  }
  wg_barrier();
  STAMP(2);
  if (wave == 0) {
#pragma unroll
    for (int h = 0; h < 2; ++h) {
      const int vI = lane + 64 * h;
      const char* s = lds + L_REDQ + vI * 16;
      f32x4 a = *(const f32x4*)s + *(const f32x4*)(s + 2048) + *(const f32x4*)(s + 4096) + *(const f32x4*)(s + 6144);
      st_sc(__builtin_bit_cast(u32x4, a), rs_ws, WS_QP + (u32)((xcd * 4 + (vI >> 5)) * 4096 + r * 128 + (vI & 31) * 4) * 4, 0);
    }
  }
  SB();

  u32x4 kb[4][8];
  const u32 vok = (u32)wave * 32768 + lane * 16;
  KEEP(wg[7]);
  SB();
#pragma unroll
  for (int j = 0; j < 8; ++j) kb[0][j] = ld_nt(rs_k, vok, j * 1024);
  SB();
  {
    f32x4 acc[8];
#pragma unroll
    for (int i = 0; i < 8; ++i) acc[i] = mfma(xf, colfrag(wg, i), f32x4{0.f, 0.f, 0.f, 0.f});
    if (g == 0) {
      char* d = lds + L_REDGU + wave * 4096 + c * 128;
#pragma unroll
      for (int i = 0; i < 8; ++i) *(f32x4*)(d + i * 16) = acc[i];
    }
  }
  KEEP(wu[7]);
  SB();
#pragma unroll
  for (int j = 0; j < 8; ++j) kb[1][j] = ld_nt(rs_k, vok, 8192 + j * 1024);
  SB();
  {
    f32x4 acc[8];
#pragma unroll
    for (int i = 0; i < 8; ++i) acc[i] = mfma(xf, colfrag(wu, i), f32x4{0.f, 0.f, 0.f, 0.f});
    if (g == 0) {
      char* d = lds + L_REDGU + wave * 4096 + 2048 + c * 128;
#pragma unroll
      for (int i = 0; i < 8; ++i) *(f32x4*)(d + i * 16) = acc[i];
    }
  }
  wg_barrier();
  STAMP(3);
  {
    const char* s = lds + L_REDGU + tid * 16;
    f32x4 a = *(const f32x4*)s + *(const f32x4*)(s + 4096) + *(const f32x4*)(s + 8192) + *(const f32x4*)(s + 12288);
    st_sc(tag4(a, T12), rs_wsf, WS_GU + (u32)((xcd * 2 + (tid >> 7)) * DF + r * 128 + (tid & 127)) * 16, 0);
  }
  SB();

  char* kl = lds + L_K + wave * 32768;
#define KSTORE(G)                                                                                   \
  _Pragma("unroll") for (int j = 0; j < 8; ++j) {                                                   \
    const int t_ = (G) * 8 + j;                                                                     \
    *(u32x4*)(kl + (t_ * 4 + g) * 256 + ((c ^ (g * 4 + (t_ & 3))) * 16)) = kb[G][j];                \
  }
  KEEP(kb[0][7]);
  if (tid == 0) st_sc64(u32x2{(u32)epoch, (u32)(epoch >> 32)}, rs_ws, WS_QF + (u32)(r * 8 + xcd) * 8, 0);
  SB();
#pragma unroll
  for (int j = 0; j < 8; ++j) kb[2][j] = ld_nt(rs_k, vok, 16384 + j * 1024);
  SB();
  KSTORE(0)
  KEEP(kb[1][7]);
  SB();
#pragma unroll
  for (int j = 0; j < 8; ++j) kb[3][j] = ld_nt(rs_k, vok, 24576 + j * 1024);
  SB();
  KSTORE(1)
  STAMP(4);
  u32x2 qf = ld_sc64(rs_ws, WS_QF + (u32)(kvh * 64 + lane) * 8, 0);
  SB();
  u32x4 vb[4][8];
  KEEP(kb[2][7]);
  SB();
#pragma unroll
  for (int j = 0; j < 8; ++j) vb[0][j] = ld_nt(rs_v, vok, j * 1024);
  SB();
  KSTORE(2)
  KEEP(kb[3][7]);
  SB();
  if (!__all(qf[0] == (u32)epoch && qf[1] == (u32)(epoch >> 32))) {
    for (int spins = 0; spins < SPIN_CAP; ++spins) {
      asm volatile("" ::: "memory");
      const u32x2 q2 = ld_sc64(rs_ws, WS_QF + (u32)(kvh * 64 + lane) * 8, 0);
      if (__all(q2[0] == (u32)epoch && q2[1] == (u32)(epoch >> 32))) break;
    }
  }
  asm volatile("" ::: "memory");
  u32x4 qp[8];
#pragma unroll
  for (int s = 0; s < 8; ++s) qp[s] = ld_sc(rs_ws, WS_QP + (u32)(b_att * 4096 + kvh * 1024 + tid * 4) * 4, s * 65536);
#pragma unroll
  for (int j = 0; j < 8; ++j) vb[1][j] = ld_nt(rs_v, vok, 8192 + j * 1024);
  SB();
  KSTORE(3)
  STAMP(5);
  {
    f32x4 a = __builtin_bit_cast(f32x4, qp[0]);
#pragma unroll
    for (int s = 1; s < 8; ++s) a += __builtin_bit_cast(f32x4, qp[s]);
    *(u32x2*)(lds + L_QS + tid * 8) = u32x2{cvt_pk(a[0], a[1]), cvt_pk(a[2], a[3])};
  }
  wg_barrier();
  STAMP(6);

  u32x4 pf[4];
  float m_w, l_w;
  {
    u32x4 qfr[4];
#pragma unroll
    for (int s = 0; s < 4; ++s) qfr[s] = *(const u32x4*)(lds + L_QS + (c & 7) * 256 + (4 * s + g) * 16);
    const int rho = (c & 3) * 4 + (c >> 2);
    float sc[8][4];
#pragma unroll
    for (int a = 0; a < 8; ++a) {
      f32x4 acc = {0.f, 0.f, 0.f, 0.f};
#pragma unroll
      for (int s = 0; s < 4; ++s) {
        const u32x4 kf = *(const u32x4*)(kl + (a * 16 + rho) * 256 + (((4 * s + g) ^ c) * 16));
        acc = mfma(kf, qfr[s], acc);
      }
#pragma unroll
      for (int i = 0; i < 4; ++i) sc[a][i] = rbf(rbf(acc[i]) * 0.08838834764831845f);
    }
    float m = sc[0][0];
#pragma unroll
    for (int a = 0; a < 8; ++a)
#pragma unroll
      for (int i = 0; i < 4; ++i) m = fmaxf(m, sc[a][i]);
    m = xrow_max(m);
    float l = 0.f;
#pragma unroll
    for (int a = 0; a < 8; ++a) {
#pragma unroll
      for (int i = 0; i < 4; ++i) { sc[a][i] = fexp(sc[a][i] - m); l += sc[a][i]; }
    }
#pragma unroll
    for (int blk = 0; blk < 4; ++blk) {
      pf[blk][0] = cvt_pk(sc[2 * blk][0], sc[2 * blk][1]);
      pf[blk][1] = cvt_pk(sc[2 * blk][2], sc[2 * blk][3]);
      pf[blk][2] = cvt_pk(sc[2 * blk + 1][0], sc[2 * blk + 1][1]);
      pf[blk][3] = cvt_pk(sc[2 * blk + 1][2], sc[2 * blk + 1][3]);
    }
    m_w = m; l_w = xrow_sum(l);
  }
  STAMP(7);

  f32x4 oacc[8];
#pragma unroll
  for (int i = 0; i < 8; ++i) oacc[i] = f32x4{0.f, 0.f, 0.f, 0.f};
  u32x4 wd[8], wo[8], gp[8];
  KEEP(vb[0][7]);
  SB();
  const u32 gu_off = WS_GU + (u32)((lane >> 5) * DF + rg * 128 + wave * 32 + (lane & 31)) * 16;
#pragma unroll
  for (int s = 0; s < 8; ++s) gp[s] = ld_sc(rs_wsf, gu_off, s * (2 * DF * 16));
#pragma unroll
  for (int j = 0; j < 8; ++j) vb[2][j] = ld_nt(rs_v, vok, 16384 + j * 1024);
  SB();
#pragma unroll
  for (int i = 0; i < 8; ++i) oacc[i] = mfma(colfrag(vb[0], i), pf[0], oacc[i]);
  KEEP(vb[1][7]);
  SB();
#pragma unroll
  for (int j = 0; j < 8; ++j) vb[3][j] = ld_nt(rs_v, vok, 24576 + j * 1024);
  SB();
#pragma unroll
  for (int i = 0; i < 8; ++i) oacc[i] = mfma(colfrag(vb[1], i), pf[1], oacc[i]);
  {
    u32 bad = 0;
#pragma unroll
    for (int s = 0; s < 8; ++s) bad |= tagbad(gp[s], T12);
    if (has_ff && !__all(bad == 0)) {
      for (int spins = 0; spins < SPIN_CAP; ++spins) {
        asm volatile("" ::: "memory");
#pragma unroll
        for (int s = 0; s < 8; ++s) gp[s] = ld_sc(rs_wsf, gu_off, s * (2 * DF * 16));
        bad = 0;
#pragma unroll
        for (int s = 0; s < 8; ++s) bad |= tagbad(gp[s], T12);
        if (__all(bad == 0)) break;
      }
    }
  }
  {
    f32x4 a = untag(gp[0]);
#pragma unroll
    for (int s = 1; s < 8; ++s) a += untag(gp[s]);
#pragma unroll
    for (int b = 0; b < 4; ++b) {
      const float v = rbf(a[b]);
      auto t = __builtin_amdgcn_permlane32_swap(__float_as_uint(v), __float_as_uint(v), false, false);
      const float gv = __uint_as_float(t[0]), uv = __uint_as_float(t[1]);
      const float inner = 0.7978845608028654f * (gv + 0.044715f * gv * gv * gv);
      const float th = 1.f - 2.f / (fexp(2.f * inner) + 1.f);
      const float ge = rbf(0.5f * gv * (1.f + th));
      const u32 hv = cvt_pk(ge * uv, 0.f);
      if (lane < 32) *(unsigned short*)(lds + L_HS + b * 256 + (wave * 32 + lane) * 2) = (unsigned short)hv;
    }
  }
  KEEP(vb[2][7]);
  SB();
#pragma unroll
  for (int j = 0; j < 8; ++j) wd[j] = ld_nt(rs_wd, vo2, j * 2048);
  SB();
#pragma unroll
  for (int i = 0; i < 8; ++i) oacc[i] = mfma(colfrag(vb[2], i), pf[2], oacc[i]);
  KEEP(vb[3][7]);
  SB();
#pragma unroll
  for (int j = 0; j < 8; ++j) wo[j] = ld_nt(rs_wo, vo2, j * 2048);
  SB();
#pragma unroll
  for (int i = 0; i < 8; ++i) oacc[i] = mfma(colfrag(vb[3], i), pf[3], oacc[i]);
  STAMP(8);
  if (c < 8) {
    char* d = kl + LK_MRG + c * 512 + g * 128;
#pragma unroll
    for (int q = 0; q < 4; ++q) {
      *(f32x4*)(d + q * 32) = f32x4{oacc[0][q], oacc[1][q], oacc[2][q], oacc[3][q]};
      *(f32x4*)(d + q * 32 + 16) = f32x4{oacc[4][q], oacc[5][q], oacc[6][q], oacc[7][q]};
    }
    if (g == 0) *(float2*)(lds + L_ML + (wave * 8 + c) * 8) = make_float2(m_w, l_w);
  }
  wg_barrier();
  {
    const int hh = tid >> 5, d4 = tid & 31;
    float2 ml[4];
#pragma unroll
    for (int w = 0; w < 4; ++w) ml[w] = *(const float2*)(lds + L_ML + (w * 8 + hh) * 8);
    const float M = fmaxf(fmaxf(ml[0].x, ml[1].x), fmaxf(ml[2].x, ml[3].x));
    f32x4 o = {0.f, 0.f, 0.f, 0.f};
    float L = 0.f;
#pragma unroll
    for (int w = 0; w < 4; ++w) {
      const float f = fexp(ml[w].x - M);
      L = fmaf(f, ml[w].y, L);
      o += f * *(const f32x4*)(lds + L_K + w * 32768 + LK_MRG + hh * 512 + d4 * 16);
    }
    const u32 slot = (u32)((b_att * 32 + kvh * 8 + hh) * 16 + split);
    st_sc(tag4(o, T12), rs_ws, WS_OP + slot * 512 + d4 * 16, 0);
    if (d4 == 0) st_sc(tag4(f32x4{M, L, 0.f, 0.f}, T12), rs_ws, WS_ML + slot * 16, 0);
  }
  STAMP(9);
  SB();

  const u32x4 hf = *(const u32x4*)(lds + L_HS + (c & 3) * 256 + wave * 64 + g * 16);
  KEEP(wd[7]);
  SB();
  u32x4 og[8], mlv;
  const u32 og_off = WS_OP + (u32)((wave * 32 + head_o) * 16 + (lane >> 5) * 8) * 512 + (lane & 31) * 16;
  const u32 ml_off = WS_ML + (u32)((wave * 32 + head_o) * 16 + c) * 16;
#pragma unroll
  for (int s = 0; s < 8; ++s) og[s] = ld_sc(rs_ws, og_off, s * 512);
  mlv = ld_sc(rs_ws, ml_off, 0);
  SB();
  {
    f32x4 acc[8];
#pragma unroll
    for (int i = 0; i < 8; ++i) acc[i] = mfma(hf, colfrag(wd, i), f32x4{0.f, 0.f, 0.f, 0.f});
    if (g == 0) {
      char* d = kl + LK_RD + c * 128;
#pragma unroll
      for (int i = 0; i < 8; ++i) *(f32x4*)(d + i * 16) = acc[i];
    }
  }
  wg_barrier();
  if (tid < 128) {
    const char* s = lds + L_K + LK_RD + tid * 16;
    f32x4 a = *(const f32x4*)s + *(const f32x4*)(s + 32768) + *(const f32x4*)(s + 65536) + *(const f32x4*)(s + 98304);
    st_sc(tag4(a, T12), rs_wsf, WS_FP + (u32)((32 + rg) * 1024 + cgd * 128 + tid) * 16, 0);
  }
  STAMP(10);
  SB();
  {
    int spins = 0;
    for (;;) {
      u32 bad = tagbad(mlv, T12);
#pragma unroll
      for (int s = 0; s < 8; ++s) bad |= tagbad(og[s], T12);
      if (__all(bad == 0) || ++spins > SPIN_CAP) break;
      asm volatile("" ::: "memory");
#pragma unroll
      for (int s = 0; s < 8; ++s) og[s] = ld_sc(rs_ws, og_off, s * 512);
      mlv = ld_sc(rs_ws, ml_off, 0);
    }
    const f32x4 mlf = untag(mlv);
    const float M = row16_max(mlf[0]);
    const float w = fexp(mlf[0] - M);
    const float L = row16_sum(w * mlf[1]);
    f32x4 o = {0.f, 0.f, 0.f, 0.f};
#pragma unroll
    for (int s = 0; s < 8; ++s) {
      const float ws_ = __shfl(w, (lane >> 5) * 8 + s);
      o += ws_ * untag(og[s]);
    }
    const float inv = 1.f / L;
#pragma unroll
    for (int i = 0; i < 4; ++i) o[i] = x32_sum(o[i]) * inv;
    if (lane < 32) *(u32x2*)(lds + L_OS + wave * 256 + lane * 8) = u32x2{cvt_pk(o[0], o[1]), cvt_pk(o[2], o[3])};
  }
  wg_barrier();
  STAMP(11);
  {
    const u32x4 of = *(const u32x4*)(lds + L_OS + (c & 3) * 256 + wave * 64 + g * 16);
    f32x4 acc[8];
#pragma unroll
    for (int i = 0; i < 8; ++i) acc[i] = mfma(of, colfrag(wo, i), f32x4{0.f, 0.f, 0.f, 0.f});
    if (g == 0) {
      char* d = kl + LK_RO + c * 128;
#pragma unroll
      for (int i = 0; i < 8; ++i) *(f32x4*)(d + i * 16) = acc[i];
    }
  }
  wg_barrier();
  if (tid < 128) {
    const char* s = lds + L_K + LK_RO + tid * 16;
    f32x4 a = *(const f32x4*)s + *(const f32x4*)(s + 32768) + *(const f32x4*)(s + 65536) + *(const f32x4*)(s + 98304);
    st_sc(tag4(a, T12), rs_ws, WS_FP + (u32)(head_o * 1024 + cgo * 128 + tid) * 16, 0);
  }
  STAMP(12);
  SB();
  {
    const int col0 = (xcd * 32 + r) * 4;
    const int ks = tid >> 2, cc = tid & 3;
    const bool act = ks < 60;
    const u32 off = act ? WS_FP + (u32)(ks * 1024 + col0 + cc) * 16 : 0xfffffff0u;
    u32x4 fv;
    int spins = 0;
    for (;;) {
      fv = ld_sc(rs_ws, off, 0);
      const u32 bad = act ? tagbad(fv, T12) : 0u;
      if (__all(bad == 0) || ++spins > SPIN_CAP) break;
      asm volatile("" ::: "memory");
    }
    f32x4 a = untag(fv);
    if (!act) a = f32x4{0.f, 0.f, 0.f, 0.f};
#pragma unroll
    for (int i = 0; i < 4; ++i) {
      float t = a[i];
      t += dpp<0x124>(t);
      t += dpp<XOR8>(t);
      a[i] = xrow_sum(t);
    }
    if (lane < 4) *(f32x4*)(lds + L_K + LK_FIN + (wave * 4 + lane) * 16) = a;
    wg_barrier();
    if (tid < 16) {
      const int b = tid >> 2, cc2 = tid & 3;
      const float* fs = (const float*)(lds + L_K + LK_FIN);
      const float s = rbf(fs[(0 * 4 + cc2) * 4 + b] + fs[(1 * 4 + cc2) * 4 + b]);
      const float d = rbf(fs[(2 * 4 + cc2) * 4 + b] + fs[(3 * 4 + cc2) * 4 + b]);
      const float gt = rbf(d / (1.f + fexp(-d)));
      const unsigned short xin = *(const unsigned short*)(p.x + (b * DM + col0 + cc2) * 2);
      const float o = bf_lo(xin) + rbf(s * gt);
      *(unsigned short*)(p.out + (b * DM + col0 + cc2) * 2) = (unsigned short)cvt_pk(o, o);
    }
    if (bid == 0 && tid == 0) st_sc64(u32x2{(u32)epoch, (u32)(epoch >> 32)}, rs_ws, WS_E, 0);
  }
  KEEP(cw0);
  asm volatile(".Lfdk_end:");
  STAMP(13);
}

extern "C" void kernel_launch(void* const* d_in, const int* in_sizes, int n_in, void* d_out, int out_size, void* d_ws,
                              size_t ws_size, hipStream_t stream) {
  Params p;
  p.x = (const char*)d_in[0]; p.k = (const char*)d_in[1]; p.v = (const char*)d_in[2];
  p.wq = (const char*)d_in[3]; p.wo = (const char*)d_in[4];
  p.wg = (const char*)d_in[6]; p.wu = (const char*)d_in[7]; p.wd = (const char*)d_in[8];
  p.out = (char*)d_out; p.ws = (char*)d_ws;
  (void)hipFuncSetAttribute((const void*)fused_decode_kernel, hipFuncAttributeMaxDynamicSharedMemorySize, L_END);
  fused_decode_kernel<<<NWG, NTHR, L_END, stream>>>(p);
}
```

```cpp
#include <hip/hip_runtime.h>
#include <stdint.h>

typedef unsigned u32;
typedef unsigned long long u64;
typedef u32 u32x4 __attribute__((ext_vector_type(4)));
typedef u32 u32x2 __attribute__((ext_vector_type(2)));
typedef float f32x4 __attribute__((ext_vector_type(4)));
typedef __bf16 bf16x8 __attribute__((ext_vector_type(8)));
typedef __amdgpu_buffer_rsrc_t rsrc_t;

constexpr int NB = 4, DM = 1024, HQ = 32, HKV = 4, SK = 8192, DH = 128, DF = 3584;
constexpr int NWG = 256, NTHR = 256;

constexpr u32 WS_E = 0;
constexpr u32 WS_QC = 4096;
constexpr u32 WS_QP = 32768;
constexpr u32 WS_GU = WS_QP + 8 * 4 * 4096 * 4;
constexpr u32 WS_OP = WS_GU + 4 * 2 * DF * 16;
constexpr u32 WS_ML = WS_OP + 4 * 32 * 16 * 256;
constexpr u32 WS_FP = WS_ML + 4 * 32 * 16 * 16;
constexpr u32 WS_END = WS_FP + 60 * 1024 * 16;

constexpr int L_K = 0;
constexpr int L_REDQ = 131072;
constexpr int L_XN = L_REDQ + 8192;
constexpr int L_QS = L_XN + 2048;
constexpr int L_OS = L_QS + 8 * 272;
constexpr int L_ML = L_OS + 1024;
constexpr int L_END = L_ML + 256;
constexpr int LK_MRG = 0;
constexpr int LK_RF = 8192;
constexpr int LK_RD = 10240;
constexpr int LK_RO = 12288;
constexpr int LK_HS = 14336;
constexpr int LK_FIN = 16384;

struct Params {
  const char* x; const char* k; const char* v; const char* wq; const char* wo;
  const char* wg; const char* wu; const char* wd; char* out; char* ws;
};

#define SB() __builtin_amdgcn_sched_barrier(0)
#define KEEP(x) do { SB(); asm volatile("" ::"v"(x)); } while (0)

__device__ __forceinline__ rsrc_t mk(const char* p, u32 bytes) {
  return __builtin_amdgcn_make_buffer_rsrc((void*)p, 0, bytes, 0x00020000);
}
__device__ __forceinline__ u32x4 ld_nt(rsrc_t r, u32 voff, u32 soff) { return __builtin_amdgcn_raw_buffer_load_b128(r, voff, soff, 2); }
__device__ __forceinline__ u32x4 ld_pl(rsrc_t r, u32 voff, u32 soff) { return __builtin_amdgcn_raw_buffer_load_b128(r, voff, soff, 0); }
__device__ __forceinline__ u32x4 ld_sc(rsrc_t r, u32 voff, u32 soff) { return __builtin_amdgcn_raw_buffer_load_b128(r, voff, soff, 16); }
__device__ __forceinline__ u32x2 ld_sc64(rsrc_t r, u32 voff, u32 soff) { return __builtin_amdgcn_raw_buffer_load_b64(r, voff, soff, 16); }
__device__ __forceinline__ void st_sc(u32x4 v, rsrc_t r, u32 voff, u32 soff) { __builtin_amdgcn_raw_buffer_store_b128(v, r, voff, soff, 16); }
__device__ __forceinline__ void st_sc64(u32x2 v, rsrc_t r, u32 voff, u32 soff) { __builtin_amdgcn_raw_buffer_store_b64(v, r, voff, soff, 16); }

__device__ __forceinline__ void wg_barrier() {
  asm volatile("s_waitcnt lgkmcnt(0)\n\ts_barrier" ::: "memory");
}
__device__ __forceinline__ float bf_lo(u32 v) { return __uint_as_float(v << 16); }
__device__ __forceinline__ float bf_hi(u32 v) { return __uint_as_float(v & 0xffff0000u); }
typedef __bf16 bf16x2 __attribute__((ext_vector_type(2)));
typedef float f32x2 __attribute__((ext_vector_type(2)));
__device__ __forceinline__ u32 cvt_pk(float lo, float hi) {
  return __builtin_bit_cast(u32, __builtin_convertvector(f32x2{lo, hi}, bf16x2));
}
__device__ __forceinline__ float rbf(float f) { return bf_lo(cvt_pk(f, f)); }

template <int CTRL> __device__ __forceinline__ float dpp(float x) {
  return __builtin_bit_cast(float, __builtin_amdgcn_mov_dpp(__builtin_bit_cast(int, x), CTRL, 0xf, 0xf, true));
}
constexpr int XOR1 = 0xB1, XOR2 = 0x4E, XOR7 = 0x141, XOR8 = 0x128;
__device__ __forceinline__ float xrow_sum(float x) {
  auto s = __builtin_amdgcn_permlane16_swap(__float_as_uint(x), __float_as_uint(x), false, false);
  x = __uint_as_float(s[0]) + __uint_as_float(s[1]);
  auto t = __builtin_amdgcn_permlane32_swap(__float_as_uint(x), __float_as_uint(x), false, false);
  return __uint_as_float(t[0]) + __uint_as_float(t[1]);
}
__device__ __forceinline__ float xrow_max(float x) {
  auto s = __builtin_amdgcn_permlane16_swap(__float_as_uint(x), __float_as_uint(x), false, false);
  x = fmaxf(__uint_as_float(s[0]), __uint_as_float(s[1]));
  auto t = __builtin_amdgcn_permlane32_swap(__float_as_uint(x), __float_as_uint(x), false, false);
  return fmaxf(__uint_as_float(t[0]), __uint_as_float(t[1]));
}
__device__ __forceinline__ float x32_sum(float x) {
  auto t = __builtin_amdgcn_permlane32_swap(__float_as_uint(x), __float_as_uint(x), false, false);
  return __uint_as_float(t[0]) + __uint_as_float(t[1]);
}
__device__ __forceinline__ float wave_sum(float x) {
  x += dpp<XOR1>(x); x += dpp<XOR2>(x); x += dpp<XOR7>(x); x += dpp<XOR8>(x);
  return xrow_sum(x);
}
__device__ __forceinline__ float row16_max(float x) {
  x = fmaxf(x, dpp<XOR1>(x)); x = fmaxf(x, dpp<XOR2>(x)); x = fmaxf(x, dpp<XOR7>(x)); x = fmaxf(x, dpp<XOR8>(x));
  return x;
}
__device__ __forceinline__ float row16_sum(float x) {
  x += dpp<XOR1>(x); x += dpp<XOR2>(x); x += dpp<XOR7>(x); x += dpp<XOR8>(x);
  return x;
}
__device__ __forceinline__ float fexp(float x) { return __builtin_amdgcn_exp2f(x * 1.4426950408889634f); }

__device__ __forceinline__ f32x4 mfma(u32x4 a, u32x4 b, f32x4 c) {
  return __builtin_amdgcn_mfma_f32_16x16x32_bf16(__builtin_bit_cast(bf16x8, a), __builtin_bit_cast(bf16x8, b), c, 0, 0, 0);
}
__device__ __forceinline__ u32x4 colfrag(const u32x4 (&w)[8], int i) {
  const u32 sel = (i & 1) ? 0x07060302u : 0x05040100u;
  const int d = i >> 1;
  u32x4 f;
  f[0] = __builtin_amdgcn_perm(w[1][d], w[0][d], sel);
  f[1] = __builtin_amdgcn_perm(w[3][d], w[2][d], sel);
  f[2] = __builtin_amdgcn_perm(w[5][d], w[4][d], sel);
  f[3] = __builtin_amdgcn_perm(w[7][d], w[6][d], sel);
  return f;
}
__device__ __forceinline__ u32 tag1(float v, u32 t) { return ((__float_as_uint(v) + 0x800u) & 0xfffff000u) | t; }
__device__ __forceinline__ u32x4 tag4(f32x4 v, u32 t) { u32x4 r = {tag1(v[0], t), tag1(v[1], t), tag1(v[2], t), tag1(v[3], t)}; return r; }
__device__ __forceinline__ u32 tagbad(u32x4 v, u32 t) { return ((v[0] ^ t) | (v[1] ^ t) | (v[2] ^ t) | (v[3] ^ t)) & 0xfffu; }
__device__ __forceinline__ f32x4 untag(u32x4 v) {
  f32x4 r = {__uint_as_float(v[0] & 0xfffff000u), __uint_as_float(v[1] & 0xfffff000u), __uint_as_float(v[2] & 0xfffff000u), __uint_as_float(v[3] & 0xfffff000u)};
  return r;
}
constexpr int SPIN_CAP = 1 << 18;
constexpr u32 OOB = 0x80000000u;
template <int CTRL> __device__ __forceinline__ u32 dppu(u32 x) { return (u32)__builtin_amdgcn_mov_dpp((int)x, CTRL, 0xf, 0xf, true); }
__device__ __forceinline__ u32 half_xor(u32 x) {
  x ^= dppu<XOR1>(x); x ^= dppu<XOR2>(x); x ^= dppu<XOR7>(x); x ^= dppu<XOR8>(x);
  auto t = __builtin_amdgcn_permlane16_swap(x, x, false, false);
  return t[0] ^ t[1];
}
__device__ __forceinline__ u32 row_xor(u32 x) {
  x ^= dppu<XOR1>(x); x ^= dppu<XOR2>(x); x ^= dppu<XOR7>(x); x ^= dppu<XOR8>(x);
  return x;
}
__device__ __forceinline__ float frcp(float x) { return __builtin_amdgcn_rcpf(x); }
__device__ __forceinline__ void tile_store(char* d, const f32x4 (&acc)[8], int c) {
#pragma unroll
  for (int i = 0; i < 8; ++i) *(f32x4*)(d + (c * 8 + (i ^ (c & 7))) * 16) = acc[i];
}
__device__ __forceinline__ int tile_off(int col) { return (col ^ ((col >> 3) & 7)) * 16; }

#define STAMP(i) do {} while (0)
#define STAMP_DECL
#define STAMP_FLUSH do {} while (0)

__global__ void __launch_bounds__(NTHR, 1) fused_decode_kernel(Params p) {
  extern __shared__ __attribute__((aligned(16))) char lds[];
  const int tid = threadIdx.x, lane = tid & 63;
  const int wave = __builtin_amdgcn_readfirstlane(tid >> 6);
  const int c = lane & 15, g = lane >> 4;
  const int bid = blockIdx.x, xcd = bid & 7, r = bid >> 3;
  STAMP_DECL
  STAMP(0);

  const int pair = xcd * 2 + (r >> 4), b_att = pair >> 2, kvh = pair & 3, split = r & 15;
  const rsrc_t rs_ws = mk(p.ws, WS_END);
  const rsrc_t rs_x = mk(p.x, NB * DM * 2);
  const rsrc_t rs_wq = mk(p.wq + (size_t)(128 * xcd) * 8192 + r * 256, 128 * 8192 - r * 256);
  const rsrc_t rs_k = mk(p.k + ((size_t)pair * SK + split * 512) * 256, 512 * 256);

  u32x2 e2 = ld_sc64(rs_ws, WS_E, 0);
  u32x4 x0 = ld_pl(rs_x, wave * 2048 + lane * 32, 0);
  u32x4 x1 = ld_pl(rs_x, wave * 2048 + lane * 32 + 16, 0);
  const u32x2 xs = __builtin_amdgcn_raw_buffer_load_b64(rs_x, wave * 2048 + (xcd >> 1) * 512 + lane * 8, 0, 0);
  u32 cw0;
  {
    u64 pc; u32 len;
    asm volatile("s_getpc_b64 %0\n.Lfdk_here:\n\ts_mov_b32 %1, .Lfdk_end-.Lfdk_here" : "=s"(pc), "=s"(len));
    len = (int)len > 0 ? len : 0u;
    const rsrc_t rs_c = mk((const char*)pc, len);
    cw0 = __builtin_amdgcn_raw_buffer_load_b32(rs_c, (wave * 64 + lane) * 128, 0, 0);
  }
  SB();
  u32x4 wq[8], kb[32], vb[4][8], fa[8], fb[8], wo[8], wd[8];
  const u32 vo8 = (u32)(wave * 32 + g * 8) * 8192 + c * 16;
  const u32 vok = (u32)wave * 32768 + lane * 16;
#define ISSUE(n)                                                                                              \
  do {                                                                                                        \
    SB();                                                                                                     \
    _Pragma("unroll") for (int j_ = 0; j_ < 4; ++j_) {                                                        \
      if ((n) < 2) wq[(n) * 4 + j_] = ld_nt(rs_wq, vo8, ((n) * 4 + j_) * 8192);                               \
      else if ((n) < 10) kb[((n) - 2) * 4 + j_] = ld_nt(rs_k, vok, (((n) - 2) * 4 + j_) * 1024);              \
      else if ((n) < 18) vb[((n) - 10) / 2][(((n) - 10) & 1) * 4 + j_] = ld_nt(rs_v, vok, (((n) - 10) * 4 + j_) * 1024); \
      else if ((n) < 20) fa[((n) - 18) * 4 + j_] = ld_nt(rs_wf, vo7, (((n) - 18) * 4 + j_) * 7168);           \
      else if ((n) < 22) fb[((n) - 20) * 4 + j_] = ld_nt(rs_wf, vo7, (128 + ((n) - 20) * 4 + j_) * 7168);     \
      else if ((n) < 24) wo[((n) - 22) * 4 + j_] = ld_nt(rs_wo, vo2, (((n) - 22) * 4 + j_) * 2048);           \
      else wd[((n) - 24) * 4 + j_] = ld_nt(rs_wd, vo2, (((n) - 24) * 4 + j_) * 2048);                         \
    }                                                                                                         \
    SB();                                                                                                     \
  } while (0)
#define ISSUE_A(n)                                                                                            \
  do {                                                                                                        \
    SB();                                                                                                     \
    _Pragma("unroll") for (int j_ = 0; j_ < 4; ++j_) {                                                        \
      if ((n) < 2) wq[(n) * 4 + j_] = ld_nt(rs_wq, vo8, ((n) * 4 + j_) * 8192);                               \
      else kb[((n) - 2) * 4 + j_] = ld_nt(rs_k, vok, (((n) - 2) * 4 + j_) * 1024);                            \
    }                                                                                                         \
    SB();                                                                                                     \
  } while (0)
#define WAITG(n)                                                                                              \
  do {                                                                                                        \
    if ((n) < 2) KEEP(wq[(n) * 4 + 3]);                                                                       \
    else if ((n) < 10) KEEP(kb[((n) - 2) * 4 + 3]);                                                           \
    else if ((n) < 18) KEEP(vb[((n) - 10) / 2][(((n) - 10) & 1) * 4 + 3]);                                    \
    else if ((n) < 20) KEEP(fa[((n) - 18) * 4 + 3]);                                                          \
    else if ((n) < 22) KEEP(fb[((n) - 20) * 4 + 3]);                                                          \
    else if ((n) < 24) KEEP(wo[((n) - 22) * 4 + 3]);                                                          \
    else KEEP(wd[((n) - 24) * 4 + 3]);                                                                        \
  } while (0)
  ISSUE_A(0); ISSUE_A(1); ISSUE_A(2); ISSUE_A(3);

  const bool has_ff = r < 28;
  const int cgf = r + 1 - (r + 1 >= 28 ? 28 : 0);
  const int rgo = xcd * 4 + (r >> 3), cgo = r & 7;
  const int td = xcd * 28 + r + 3 - (r + 3 >= 28 ? 28 : 0), rgd = td >> 3, cgd = td & 7;
  const rsrc_t rs_wsf = mk(p.ws, has_ff ? WS_END : 0u);
  const rsrc_t rs_v = mk(p.v + ((size_t)pair * SK + split * 512) * 256, 512 * 256);
  const rsrc_t rs_wf = mk(((xcd & 1) ? p.wu : p.wg) + (size_t)(256 * (xcd >> 1)) * 7168 + cgf * 256, has_ff ? 256 * 7168 - cgf * 256 : 0);
  const rsrc_t rs_wd = mk(p.wd + (size_t)rgd * 128 * 2048 + cgd * 256, has_ff ? 128 * 2048 - cgd * 256 : 0);
  const rsrc_t rs_wo = mk(p.wo + (size_t)rgo * 128 * 2048 + cgo * 256, 128 * 2048 - cgo * 256);
  const u32 vo7 = (u32)(wave * 32 + g * 8) * 7168 + c * 16;
  const u32 vo2 = (u32)(wave * 32 + g * 8) * 2048 + c * 16;

  {
    float v[16];
#pragma unroll
    for (int i = 0; i < 4; ++i) { v[2 * i] = bf_lo(x0[i]); v[2 * i + 1] = bf_hi(x0[i]); v[8 + 2 * i] = bf_lo(x1[i]); v[8 + 2 * i + 1] = bf_hi(x1[i]); }
    float s = 0.f;
#pragma unroll
    for (int i = 0; i < 16; ++i) s += v[i];
    const float mean = wave_sum(s) * (1.f / DM);
    float q = 0.f;
#pragma unroll
    for (int i = 0; i < 16; ++i) { const float d = v[i] - mean; q = fmaf(d, d, q); }
    const float var = wave_sum(q) * (1.f / DM);
    const float mb = rbf(mean), vb_ = rbf(var);
    const float den = rbf(sqrtf(rbf(vb_ + 1e-5f)));
    const u32x2 o2 = {cvt_pk(rbf(bf_lo(xs[0]) - mb) / den, rbf(bf_hi(xs[0]) - mb) / den),
                      cvt_pk(rbf(bf_lo(xs[1]) - mb) / den, rbf(bf_hi(xs[1]) - mb) / den)};
    *(u32x2*)(lds + L_XN + wave * 512 + lane * 8) = o2;
  }
  wg_barrier();
  STAMP(1);
  const u32x4 xfa = *(const u32x4*)(lds + L_XN + (c & 3) * 512 + wave * 64 + g * 16);
  const u32x4 xfb = *(const u32x4*)(lds + L_XN + (c & 3) * 512 + 256 + wave * 64 + g * 16);
  const u64 epoch = (((u64)(u32)__builtin_amdgcn_readfirstlane(e2[1])) << 32 | (u32)__builtin_amdgcn_readfirstlane(e2[0])) + 1ull;
  const u32 T12 = (u32)epoch & 0xfffu;

  WAITG(0); ISSUE(4);
  WAITG(1); ISSUE(5);
  {
    const u32x4 xq = (xcd & 1) ? xfb : xfa;
    f32x4 acc[8];
#pragma unroll
    for (int i = 0; i < 8; ++i) acc[i] = mfma(xq, colfrag(wq, i), f32x4{0.f, 0.f, 0.f, 0.f});
    if (g == 0) {
      char* d = lds + L_REDQ + wave * 2048;
#pragma unroll
      for (int b = 0; b < 4; ++b) {
        *(f32x4*)(d + b * 512 + ((2 * c) ^ (c >> 2 & 1)) * 16) = f32x4{acc[0][b], acc[1][b], acc[2][b], acc[3][b]};
        *(f32x4*)(d + b * 512 + ((2 * c + 1) ^ (c >> 2 & 1)) * 16) = f32x4{acc[4][b], acc[5][b], acc[6][b], acc[7][b]};
      }
    }
  }
  wg_barrier();
  STAMP(2);
  {
    const int t7 = tid & 127;
    const char* s = lds + L_REDQ + (t7 ^ (t7 >> 3 & 1)) * 16;
    const f32x4 a = *(const f32x4*)s + *(const f32x4*)(s + 2048) + *(const f32x4*)(s + 4096) + *(const f32x4*)(s + 6144);
    const u32x4 au = __builtin_bit_cast(u32x4, a);
    const u32 xr = half_xor(au[0] ^ au[1] ^ au[2] ^ au[3]);
    st_sc(au, rs_ws, tid < 128 ? WS_QP + (u32)((xcd * 4 + (t7 >> 5)) * 4096 + r * 128 + (t7 & 31) * 4) * 4 : OOB, 0);
    st_sc(u32x4{(u32)epoch, (u32)(epoch >> 32), xr, 0u}, rs_ws, (tid < 128 && (tid & 31) == 0) ? WS_QC + (u32)(((t7 >> 5) * 32 + r) * 8 + xcd) * 16 : OOB, 0);
  }
  STAMP(3);

  char* kl = lds + L_K + wave * 32768;
#define KSTORE(n)                                                                                   \
  _Pragma("unroll") for (int j = 0; j < 4; ++j) {                                                   \
    const int t_ = ((n) - 2) * 4 + j;                                                               \
    *(u32x4*)(kl + (t_ * 4 + g) * 256 + ((c ^ (g * 4 + j)) * 16)) = kb[t_];                         \
  }
  u32x4 qp[8], qc;
  const u32 qp_off = WS_QP + (u32)(b_att * 4096 + kvh * 1024 + tid * 4) * 4;
  const u32 qc_off = WS_QC + (u32)((b_att * 32 + kvh * 8 + (tid >> 5)) * 8 + (tid & 7)) * 16;
  WAITG(2); ISSUE(6); KSTORE(2)
  WAITG(3); ISSUE(7); KSTORE(3)
  WAITG(4); ISSUE(8); KSTORE(4)
  WAITG(5); ISSUE(9); KSTORE(5)
  STAMP(4);
  WAITG(6); SB(); qp[0] = ld_sc(rs_ws, qp_off, 0); qp[1] = ld_sc(rs_ws, qp_off, 65536); ISSUE(10); KSTORE(6)
  WAITG(7); SB(); qp[2] = ld_sc(rs_ws, qp_off, 2 * 65536); qp[3] = ld_sc(rs_ws, qp_off, 3 * 65536); ISSUE(11); KSTORE(7)
  WAITG(8); SB(); qp[4] = ld_sc(rs_ws, qp_off, 4 * 65536); qp[5] = ld_sc(rs_ws, qp_off, 5 * 65536); ISSUE(12); KSTORE(8)
  WAITG(9); SB(); qp[6] = ld_sc(rs_ws, qp_off, 6 * 65536); qp[7] = ld_sc(rs_ws, qp_off, 7 * 65536); qc = ld_sc(rs_ws, qc_off, 0); ISSUE(13); KSTORE(9)
  STAMP(5);
  WAITG(10); ISSUE(14);
  WAITG(11); ISSUE(15);
  WAITG(12); ISSUE(16);
#define QBAD(bad)                                                                                   \
  {                                                                                                 \
    u32 x_ = (tid & 31) < 8 ? qc[2] : 0u;                                                           \
    _Pragma("unroll") for (int s = 0; s < 8; ++s) x_ ^= qp[s][0] ^ qp[s][1] ^ qp[s][2] ^ qp[s][3];  \
    bad = half_xor(x_) | (qc[0] ^ (u32)epoch) | (qc[1] ^ (u32)(epoch >> 32));                       \
  }
  {
    u32 bad;
    QBAD(bad)
    if (!__all(bad == 0)) {
      for (int spins = 0; spins < SPIN_CAP; ++spins) {
        asm volatile("" ::: "memory");
#pragma unroll
        for (int s = 0; s < 8; ++s) qp[s] = ld_sc(rs_ws, qp_off, s * 65536);
        qc = ld_sc(rs_ws, qc_off, 0);
        QBAD(bad)
        if (__all(bad == 0)) break;
      }
    }
    f32x4 a = __builtin_bit_cast(f32x4, qp[0]);
#pragma unroll
    for (int s = 1; s < 8; ++s) a += __builtin_bit_cast(f32x4, qp[s]);
    *(u32x2*)(lds + L_QS + (tid >> 5) * 272 + (tid & 31) * 8) = u32x2{cvt_pk(a[0], a[1]), cvt_pk(a[2], a[3])};
  }
  wg_barrier();
  STAMP(6);

  u32x4 pf[2];
  float m_w, l_w;
  {
    u32x4 qlo[4], qhi[4];
#pragma unroll
    for (int s = 0; s < 4; ++s) {
      const u32x4 qv = *(const u32x4*)(lds + L_QS + (c & 7) * 272 + (4 * s + g) * 16);
      const u32x4 z = {0u, 0u, 0u, 0u};
      qlo[s] = c < 8 ? qv : z; qhi[s] = c < 8 ? z : qv;
    }
    const int rho = (c & 3) * 4 + (c >> 2);
    float sc[4][4];
#pragma unroll
    for (int u = 0; u < 2; ++u) {
#pragma unroll
      for (int a = 0; a < 2; ++a) {
        f32x4 acc = {0.f, 0.f, 0.f, 0.f};
#pragma unroll
        for (int s = 0; s < 4; ++s) {
          const u32x4 kf = *(const u32x4*)(kl + ((u * 4 + a) * 16 + rho) * 256 + (((4 * s + g) ^ c) * 16));
          acc = mfma(kf, qlo[s], acc);
        }
#pragma unroll
        for (int s = 0; s < 4; ++s) {
          const u32x4 kf = *(const u32x4*)(kl + ((u * 4 + 2 + a) * 16 + rho) * 256 + (((4 * s + g) ^ c) * 16));
          acc = mfma(kf, qhi[s], acc);
        }
        const u32 p01 = cvt_pk(acc[0], acc[1]), p23 = cvt_pk(acc[2], acc[3]);
        const u32 r01 = cvt_pk(bf_lo(p01) * 0.08838834764831845f, bf_hi(p01) * 0.08838834764831845f);
        const u32 r23 = cvt_pk(bf_lo(p23) * 0.08838834764831845f, bf_hi(p23) * 0.08838834764831845f);
        sc[u * 2 + a][0] = bf_lo(r01); sc[u * 2 + a][1] = bf_hi(r01); sc[u * 2 + a][2] = bf_lo(r23); sc[u * 2 + a][3] = bf_hi(r23);
      }
      if (u == 0) { WAITG(13); ISSUE(17); }
    }
    float m = sc[0][0];
#pragma unroll
    for (int a = 0; a < 4; ++a)
#pragma unroll
      for (int i = 0; i < 4; ++i) m = fmaxf(m, sc[a][i]);
    m = fmaxf(m, dpp<XOR8>(m));
    m = xrow_max(m);
    const float ml2 = m * 1.4426950408889634f;
    float l = 0.f;
#pragma unroll
    for (int a = 0; a < 4; ++a) {
#pragma unroll
      for (int i = 0; i < 4; ++i) { sc[a][i] = __builtin_amdgcn_exp2f(fmaf(sc[a][i], 1.4426950408889634f, -ml2)); l += sc[a][i]; }
    }
#pragma unroll
    for (int u = 0; u < 2; ++u) {
      pf[u][0] = cvt_pk(sc[2 * u][0], sc[2 * u][1]);
      pf[u][1] = cvt_pk(sc[2 * u][2], sc[2 * u][3]);
      pf[u][2] = cvt_pk(sc[2 * u + 1][0], sc[2 * u + 1][1]);
      pf[u][3] = cvt_pk(sc[2 * u + 1][2], sc[2 * u + 1][3]);
    }
    l += dpp<XOR8>(l);
    m_w = m; l_w = xrow_sum(l);
  }
  STAMP(7);

  f32x4 oa[8], ob[8];
  WAITG(14); ISSUE(18);
#pragma unroll
  for (int i = 0; i < 8; ++i) oa[i] = mfma(colfrag(vb[0], i), pf[0], f32x4{0.f, 0.f, 0.f, 0.f});
#pragma unroll
  for (int i = 0; i < 8; ++i) ob[i] = mfma(colfrag(vb[1], i), pf[0], f32x4{0.f, 0.f, 0.f, 0.f});
  WAITG(15); ISSUE(19);
#pragma unroll
  for (int i = 0; i < 8; ++i) oa[i] = mfma(colfrag(vb[2], i), pf[1], oa[i]);
  WAITG(16); ISSUE(20);
  WAITG(17); ISSUE(21);
#pragma unroll
  for (int i = 0; i < 8; ++i) ob[i] = mfma(colfrag(vb[3], i), pf[1], ob[i]);
  STAMP(8);
  {
    char* d = kl + LK_MRG + c * 512 + g * 128;
#pragma unroll
    for (int q = 0; q < 4; ++q) {
      f32x4 lo, hi;
#pragma unroll
      for (int i = 0; i < 4; ++i) { lo[i] = c < 8 ? oa[i][q] : ob[i][q]; hi[i] = c < 8 ? oa[4 + i][q] : ob[4 + i][q]; }
      *(f32x4*)(d + ((2 * q) ^ (c & 7)) * 16) = lo;
      *(f32x4*)(d + ((2 * q + 1) ^ (c & 7)) * 16) = hi;
    }
    if (lane < 8) *(float2*)(lds + L_ML + (wave * 8 + c) * 8) = make_float2(m_w, l_w);
  }
  wg_barrier();
  {
    const int hh = (tid >> 4) & 7;
    float2 ml[4];
#pragma unroll
    for (int w = 0; w < 4; ++w) ml[w] = *(const float2*)(lds + L_ML + (w * 8 + hh) * 8);
    const float M = fmaxf(fmaxf(ml[0].x, ml[1].x), fmaxf(ml[2].x, ml[3].x));
    f32x4 o0 = {0.f, 0.f, 0.f, 0.f}, o1 = {0.f, 0.f, 0.f, 0.f};
    float L = 0.f;
#pragma unroll
    for (int w = 0; w < 4; ++w) {
      const float f = fexp(ml[w].x - M);
      L = fmaf(f, ml[w].y, L);
      const char* s = lds + L_K + w * 32768 + LK_MRG + hh * 512;
      const int e0 = ((2 * c) ^ hh) * 16, e1 = ((2 * c + 1) ^ hh) * 16;
      o0 += f * (*(const f32x4*)(s + e0) + *(const f32x4*)(s + e0 + 4096));
      o1 += f * (*(const f32x4*)(s + e1) + *(const f32x4*)(s + e1 + 4096));
    }
    const float inv = frcp(L);
    o0 *= inv; o1 *= inv;
    const u32x4 pk = {cvt_pk(o0[0], o0[1]), cvt_pk(o0[2], o0[3]), cvt_pk(o1[0], o1[1]), cvt_pk(o1[2], o1[3])};
    const u32 xr = row_xor(pk[0] ^ pk[1] ^ pk[2] ^ pk[3]) ^ __float_as_uint(M) ^ __float_as_uint(L) ^ (u32)(epoch >> 32);
    const u32 slot = (u32)((b_att * 32 + kvh * 8 + hh) * 16 + split);
    st_sc(pk, rs_ws, tid < 128 ? WS_OP + slot * 256 + c * 16 : OOB, 0);
    st_sc(u32x4{__float_as_uint(M), __float_as_uint(L), xr, (u32)epoch}, rs_ws, (tid < 128 && c == 0) ? WS_ML + slot * 16 : OOB, 0);
  }
  STAMP(9);

  u32x4 og[4], mlv;
  const u32 og_off = WS_OP + (u32)((wave * 32 + rgo) * 16 + g * 4) * 256 + c * 16;
  const u32 ml_off = WS_ML + (u32)((wave * 32 + rgo) * 16 + ((c + g * 4) & 15)) * 16;
  {
    f32x4 acc[8];
    WAITG(18); ISSUE(22);
    WAITG(19); ISSUE(23);
#pragma unroll
    for (int i = 0; i < 8; ++i) acc[i] = mfma(xfa, colfrag(fa, i), f32x4{0.f, 0.f, 0.f, 0.f});
    WAITG(20); ISSUE(24);
    WAITG(21); SB();
#pragma unroll
    for (int s = 0; s < 4; ++s) og[s] = ld_sc(rs_ws, og_off, s * 256);
    mlv = ld_sc(rs_ws, ml_off, 0);
    ISSUE(25);
#pragma unroll
    for (int i = 0; i < 8; ++i) acc[i] = mfma(xfb, colfrag(fb, i), acc[i]);
    if (g == 0) tile_store(kl + LK_RF, acc, c);
  }
  wg_barrier();
  {
    const char* s = lds + L_K + LK_RF + tile_off(tid & 127);
    f32x4 a = *(const f32x4*)s + *(const f32x4*)(s + 32768) + *(const f32x4*)(s + 65536) + *(const f32x4*)(s + 98304);
    st_sc(tag4(a, T12), rs_wsf, tid < 128 ? WS_GU + (u32)(xcd * DF + cgf * 128 + tid) * 16 : OOB, 0);
  }
  STAMP(10);
  SB();
  u32x4 of_[8];
#pragma unroll
  for (int i = 0; i < 8; ++i) of_[i] = colfrag(wo, i);
#define OBAD(bad)                                                                                                  \
  {                                                                                                                \
    const u32 want = mlv[2] ^ mlv[0] ^ mlv[1] ^ (u32)(epoch >> 32);              \
    bad = mlv[3] ^ (u32)epoch;                                                                                     \
    bad |= row_xor(og[0][0] ^ og[0][1] ^ og[0][2] ^ og[0][3]) ^ dppu<0x150>(want);                                 \
    bad |= row_xor(og[1][0] ^ og[1][1] ^ og[1][2] ^ og[1][3]) ^ dppu<0x151>(want);                                 \
    bad |= row_xor(og[2][0] ^ og[2][1] ^ og[2][2] ^ og[2][3]) ^ dppu<0x152>(want);                                 \
    bad |= row_xor(og[3][0] ^ og[3][1] ^ og[3][2] ^ og[3][3]) ^ dppu<0x153>(want);                                 \
  }
  {
    u32 bad;
    OBAD(bad)
    if (!__all(bad == 0)) {
      for (int spins = 0; spins < SPIN_CAP; ++spins) {
        asm volatile("" ::: "memory");
#pragma unroll
        for (int s = 0; s < 4; ++s) og[s] = ld_sc(rs_ws, og_off, s * 256);
        mlv = ld_sc(rs_ws, ml_off, 0);
        OBAD(bad)
        if (__all(bad == 0)) break;
      }
    }
  }
  u32x4 gp[4];
  const u32 gu_off = WS_GU + (u32)((lane >> 5) * DF + rgd * 128 + wave * 32 + (lane & 31)) * 16;
  asm volatile("" ::: "memory");
#pragma unroll
  for (int s = 0; s < 4; ++s) gp[s] = ld_sc(rs_wsf, gu_off, s * (2 * DF * 16));
  SB();
  {
    const float ms = __uint_as_float(mlv[0]), ls = __uint_as_float(mlv[1]);
    const float M = row16_max(ms);
    const float wl = fexp(ms - M) * ls;
    const float w = wl * frcp(row16_sum(wl));
    const float w0 = dpp<0x150>(w), w1 = dpp<0x151>(w), w2 = dpp<0x152>(w), w3 = dpp<0x153>(w);
    u32x4 pk;
#pragma unroll
    for (int i = 0; i < 4; ++i) {
      float lo = w0 * bf_lo(og[0][i]), hi = w0 * bf_hi(og[0][i]);
      lo = fmaf(w1, bf_lo(og[1][i]), lo); hi = fmaf(w1, bf_hi(og[1][i]), hi);
      lo = fmaf(w2, bf_lo(og[2][i]), lo); hi = fmaf(w2, bf_hi(og[2][i]), hi);
      lo = fmaf(w3, bf_lo(og[3][i]), lo); hi = fmaf(w3, bf_hi(og[3][i]), hi);
      pk[i] = cvt_pk(xrow_sum(lo), xrow_sum(hi));
    }
    if (lane < 16) *(u32x4*)(lds + L_OS + wave * 256 + lane * 16) = pk;
  }
  wg_barrier();
  STAMP(11);
  {
    const u32x4 of = *(const u32x4*)(lds + L_OS + (c & 3) * 256 + wave * 64 + g * 16);
    f32x4 acc[8];
#pragma unroll
    for (int i = 0; i < 8; ++i) acc[i] = mfma(of, of_[i], f32x4{0.f, 0.f, 0.f, 0.f});
    if (g == 0) tile_store(kl + LK_RO, acc, c);
  }
  wg_barrier();
  {
    const char* s = lds + L_K + LK_RO + tile_off(tid & 127);
    f32x4 a = (*(const f32x4*)s + *(const f32x4*)(s + 32768)) + (*(const f32x4*)(s + 65536) + *(const f32x4*)(s + 98304));
    st_sc(tag4(a, T12), rs_ws, tid < 128 ? WS_FP + (u32)(rgo * 1024 + cgo * 128 + tid) * 16 : OOB, 0);
  }
  STAMP(12);
  SB();
  u32x4 df[8];
#pragma unroll
  for (int i = 0; i < 8; ++i) df[i] = colfrag(wd, i);
  {
    u32 bad = 0;
#pragma unroll
    for (int s = 0; s < 4; ++s) bad |= tagbad(gp[s], T12);
    if (has_ff && !__all(bad == 0)) {
      for (int spins = 0; spins < SPIN_CAP; ++spins) {
        asm volatile("" ::: "memory");
#pragma unroll
        for (int s = 0; s < 4; ++s) gp[s] = ld_sc(rs_wsf, gu_off, s * (2 * DF * 16));
        bad = 0;
#pragma unroll
        for (int s = 0; s < 4; ++s) bad |= tagbad(gp[s], T12);
        if (__all(bad == 0)) break;
      }
    }
    const f32x4 a = (untag(gp[0]) + untag(gp[1])) + (untag(gp[2]) + untag(gp[3]));
#pragma unroll
    for (int b = 0; b < 4; ++b) {
      const float v = rbf(a[b]);
      auto t = __builtin_amdgcn_permlane32_swap(__float_as_uint(v), __float_as_uint(v), false, false);
      const float gv = __uint_as_float(t[0]), uv = __uint_as_float(t[1]);
      const float inner = 0.7978845608028654f * (gv + 0.044715f * gv * gv * gv);
      const float th = 1.f - 2.f * frcp(fexp(2.f * inner) + 1.f);
      const float ge = rbf(0.5f * gv * (1.f + th));
      const u32 hv = cvt_pk(ge * uv, 0.f);
      if (lane < 32) *(unsigned short*)(kl + LK_HS + b * 64 + lane * 2) = (unsigned short)hv;
    }
  }
  asm volatile("s_waitcnt lgkmcnt(0)" ::: "memory");
  STAMP(13);
  {
    const u32x4 hf = *(const u32x4*)(kl + LK_HS + (c & 3) * 64 + g * 16);
    f32x4 acc[8];
#pragma unroll
    for (int i = 0; i < 8; ++i) acc[i] = mfma(hf, df[i], f32x4{0.f, 0.f, 0.f, 0.f});
    if (g == 0) tile_store(kl + LK_RD, acc, c);
  }
  wg_barrier();
  {
    const char* s = lds + L_K + LK_RD + tile_off(tid & 127);
    f32x4 a = (*(const f32x4*)s + *(const f32x4*)(s + 32768)) + (*(const f32x4*)(s + 65536) + *(const f32x4*)(s + 98304));
    st_sc(tag4(a, T12), rs_wsf, tid < 128 ? WS_FP + (u32)((32 + rgd) * 1024 + cgd * 128 + tid) * 16 : OOB, 0);
  }
  STAMP(14);
  SB();
  {
    const int col0 = (xcd * 32 + r) * 4;
    const int ks = tid >> 2, cc = tid & 3;
    const bool act = ks < 60;
    const u32 off = act ? WS_FP + (u32)(ks * 1024 + col0 + cc) * 16 : OOB;
    const unsigned short xin = *(const unsigned short*)(p.x + (((tid >> 2) & 3) * DM + col0 + cc) * 2);
    u32x4 fv;
    for (int spins = 0; spins < SPIN_CAP; ++spins) {
      fv = ld_sc(rs_ws, off, 0);
      const u32 bad = act ? tagbad(fv, T12) : 0u;
      if (__all(bad == 0)) break;
      asm volatile("" ::: "memory");
    }
    f32x4 a = untag(fv);
#pragma unroll
    for (int i = 0; i < 4; ++i) {
      float t = a[i];
      t += dpp<0x124>(t);
      t += dpp<XOR8>(t);
      a[i] = xrow_sum(t);
    }
    if (lane < 4) *(f32x4*)(lds + L_K + LK_FIN + (wave * 4 + lane) * 16) = a;
    wg_barrier();
    if (tid < 16) {
      const int b = tid >> 2, cc2 = tid & 3;
      const float* fs = (const float*)(lds + L_K + LK_FIN) + cc2 * 4 + b;
      const float s = rbf(fs[0] + fs[16]);
      const float dd = rbf(fs[32] + fs[48]);
      const float gt = rbf(dd * frcp(1.f + fexp(-dd)));
      const float o = bf_lo(xin) + rbf(s * gt);
      *(unsigned short*)(p.out + (b * DM + col0 + cc2) * 2) = (unsigned short)cvt_pk(o, o);
    }
    if (bid == 0 && tid == 0) st_sc64(u32x2{(u32)epoch, (u32)(epoch >> 32)}, rs_ws, WS_E, 0);
  }
  KEEP(cw0);
  asm volatile(".Lfdk_end:");
  STAMP(15);
  STAMP_FLUSH;
}

extern "C" void kernel_launch(void* const* d_in, const int* in_sizes, int n_in, void* d_out, int out_size, void* d_ws,
                              size_t ws_size, hipStream_t stream) {
  Params p;
  p.x = (const char*)d_in[0]; p.k = (const char*)d_in[1]; p.v = (const char*)d_in[2];
  p.wq = (const char*)d_in[3]; p.wo = (const char*)d_in[4];
  p.wg = (const char*)d_in[6]; p.wu = (const char*)d_in[7]; p.wd = (const char*)d_in[8];
  p.out = (char*)d_out; p.ws = (char*)d_ws;
  (void)hipFuncSetAttribute((const void*)fused_decode_kernel, hipFuncAttributeMaxDynamicSharedMemorySize, L_END);
  fused_decode_kernel<<<NWG, NTHR, L_END, stream>>>(p);
}
```

```cpp
#include <hip/hip_runtime.h>
#include <stdint.h>

typedef unsigned u32;
typedef unsigned long long u64;
typedef u32 u32x4 __attribute__((ext_vector_type(4)));
typedef u32 u32x2 __attribute__((ext_vector_type(2)));
typedef float f32x4 __attribute__((ext_vector_type(4)));
typedef __bf16 bf16x8 __attribute__((ext_vector_type(8)));
typedef __amdgpu_buffer_rsrc_t rsrc_t;

constexpr int NB = 4, DM = 1024, HQ = 32, HKV = 4, SK = 8192, DH = 128, DF = 3584;
constexpr int NWG = 256, NTHR = 256;

constexpr u32 WS_E = 0;
constexpr u32 WS_QC = 4096;
constexpr u32 WS_QP = 32768;
constexpr u32 WS_GU = WS_QP + 8 * 4 * 4096 * 4;
constexpr u32 WS_OP = WS_GU + 4 * 2 * DF * 16;
constexpr u32 WS_ML = WS_OP + 4 * 32 * 16 * 512;
constexpr u32 WS_FP = WS_ML + 4 * 32 * 16 * 16;
constexpr u32 WS_END = WS_FP + 120 * 1024 * 16;

constexpr int L_K = 0;
constexpr int L_REDQ = 131072;
constexpr int L_XN = L_REDQ + 8192;
constexpr int L_QS = L_XN + 2048;
constexpr int L_OS = L_QS + 8 * 272;
constexpr int L_ML = L_OS + 512;
constexpr int L_END = L_ML + 256;
constexpr int LK_MRG = 0;
constexpr int LK_RF = 8192;
constexpr int LK_RD = 10240;
constexpr int LK_RO = 12288;
constexpr int LK_HS = 14336;
constexpr int LK_FIN = 16384;

struct Params {
  const char* x; const char* k; const char* v; const char* wq; const char* wo;
  const char* wg; const char* wu; const char* wd; char* out; char* ws;
};

#define SB() __builtin_amdgcn_sched_barrier(0)
#define KEEP(x) do { SB(); asm volatile("" ::"v"(x)); } while (0)

__device__ __forceinline__ rsrc_t mk(const char* p, u32 bytes) {
  return __builtin_amdgcn_make_buffer_rsrc((void*)p, 0, bytes, 0x00020000);
}
__device__ __forceinline__ u32x4 ld_nt(rsrc_t r, u32 voff, u32 soff) { return __builtin_amdgcn_raw_buffer_load_b128(r, voff, soff, 2); }
__device__ __forceinline__ u32x4 ld_pl(rsrc_t r, u32 voff, u32 soff) { return __builtin_amdgcn_raw_buffer_load_b128(r, voff, soff, 0); }
__device__ __forceinline__ u32x4 ld_sc(rsrc_t r, u32 voff, u32 soff) { return __builtin_amdgcn_raw_buffer_load_b128(r, voff, soff, 16); }
__device__ __forceinline__ u32x2 ld_sc64(rsrc_t r, u32 voff, u32 soff) { return __builtin_amdgcn_raw_buffer_load_b64(r, voff, soff, 16); }
__device__ __forceinline__ void st_sc(u32x4 v, rsrc_t r, u32 voff, u32 soff) { __builtin_amdgcn_raw_buffer_store_b128(v, r, voff, soff, 16); }
__device__ __forceinline__ void st_sc64(u32x2 v, rsrc_t r, u32 voff, u32 soff) { __builtin_amdgcn_raw_buffer_store_b64(v, r, voff, soff, 16); }

__device__ __forceinline__ void wg_barrier() {
  asm volatile("s_waitcnt lgkmcnt(0)\n\ts_barrier" ::: "memory");
}
__device__ __forceinline__ float bf_lo(u32 v) { return __uint_as_float(v << 16); }
__device__ __forceinline__ float bf_hi(u32 v) { return __uint_as_float(v & 0xffff0000u); }
typedef __bf16 bf16x2 __attribute__((ext_vector_type(2)));
typedef float f32x2 __attribute__((ext_vector_type(2)));
__device__ __forceinline__ u32 cvt_pk(float lo, float hi) {
  return __builtin_bit_cast(u32, __builtin_convertvector(f32x2{lo, hi}, bf16x2));
}
__device__ __forceinline__ float rbf(float f) { return bf_lo(cvt_pk(f, f)); }

template <int CTRL> __device__ __forceinline__ float dpp(float x) {
  return __builtin_bit_cast(float, __builtin_amdgcn_mov_dpp(__builtin_bit_cast(int, x), CTRL, 0xf, 0xf, true));
}
constexpr int XOR1 = 0xB1, XOR2 = 0x4E, XOR7 = 0x141, XOR8 = 0x128;
__device__ __forceinline__ float xrow_sum(float x) {
  auto s = __builtin_amdgcn_permlane16_swap(__float_as_uint(x), __float_as_uint(x), false, false);
  x = __uint_as_float(s[0]) + __uint_as_float(s[1]);
  auto t = __builtin_amdgcn_permlane32_swap(__float_as_uint(x), __float_as_uint(x), false, false);
  return __uint_as_float(t[0]) + __uint_as_float(t[1]);
}
__device__ __forceinline__ float xrow_max(float x) {
  auto s = __builtin_amdgcn_permlane16_swap(__float_as_uint(x), __float_as_uint(x), false, false);
  x = fmaxf(__uint_as_float(s[0]), __uint_as_float(s[1]));
  auto t = __builtin_amdgcn_permlane32_swap(__float_as_uint(x), __float_as_uint(x), false, false);
  return fmaxf(__uint_as_float(t[0]), __uint_as_float(t[1]));
}
__device__ __forceinline__ float x32_sum(float x) {
  auto t = __builtin_amdgcn_permlane32_swap(__float_as_uint(x), __float_as_uint(x), false, false);
  return __uint_as_float(t[0]) + __uint_as_float(t[1]);
}
__device__ __forceinline__ float wave_sum(float x) {
  x += dpp<XOR1>(x); x += dpp<XOR2>(x); x += dpp<XOR7>(x); x += dpp<XOR8>(x);
  return xrow_sum(x);
}
__device__ __forceinline__ float row16_max(float x) {
  x = fmaxf(x, dpp<XOR1>(x)); x = fmaxf(x, dpp<XOR2>(x)); x = fmaxf(x, dpp<XOR7>(x)); x = fmaxf(x, dpp<XOR8>(x));
  return x;
}
__device__ __forceinline__ float row16_sum(float x) {
  x += dpp<XOR1>(x); x += dpp<XOR2>(x); x += dpp<XOR7>(x); x += dpp<XOR8>(x);
  return x;
}
__device__ __forceinline__ float fexp(float x) { return __builtin_amdgcn_exp2f(x * 1.4426950408889634f); }

__device__ __forceinline__ f32x4 mfma(u32x4 a, u32x4 b, f32x4 c) {
  return __builtin_amdgcn_mfma_f32_16x16x32_bf16(__builtin_bit_cast(bf16x8, a), __builtin_bit_cast(bf16x8, b), c, 0, 0, 0);
}
__device__ __forceinline__ u32x4 colfrag(const u32x4 (&w)[8], int i) {
  const u32 sel = (i & 1) ? 0x07060302u : 0x05040100u;
  const int d = i >> 1;
  u32x4 f;
  f[0] = __builtin_amdgcn_perm(w[1][d], w[0][d], sel);
  f[1] = __builtin_amdgcn_perm(w[3][d], w[2][d], sel);
  f[2] = __builtin_amdgcn_perm(w[5][d], w[4][d], sel);
  f[3] = __builtin_amdgcn_perm(w[7][d], w[6][d], sel);
  return f;
}
__device__ __forceinline__ u32 tag1(float v, u32 t) { return ((__float_as_uint(v) + 0x800u) & 0xfffff000u) | t; }
__device__ __forceinline__ u32x4 tag4(f32x4 v, u32 t) { u32x4 r = {tag1(v[0], t), tag1(v[1], t), tag1(v[2], t), tag1(v[3], t)}; return r; }
__device__ __forceinline__ u32 tagbad(u32x4 v, u32 t) { return ((v[0] ^ t) | (v[1] ^ t) | (v[2] ^ t) | (v[3] ^ t)) & 0xfffu; }
__device__ __forceinline__ f32x4 untag(u32x4 v) {
  f32x4 r = {__uint_as_float(v[0] & 0xfffff000u), __uint_as_float(v[1] & 0xfffff000u), __uint_as_float(v[2] & 0xfffff000u), __uint_as_float(v[3] & 0xfffff000u)};
  return r;
}
constexpr int SPIN_CAP = 1 << 18;
constexpr u32 OOB = 0xfffffff0u;
template <int CTRL> __device__ __forceinline__ u32 dppu(u32 x) { return (u32)__builtin_amdgcn_mov_dpp((int)x, CTRL, 0xf, 0xf, true); }
__device__ __forceinline__ u32 half_xor(u32 x) {
  x ^= dppu<XOR1>(x); x ^= dppu<XOR2>(x); x ^= dppu<XOR7>(x); x ^= dppu<XOR8>(x);
  auto t = __builtin_amdgcn_permlane16_swap(x, x, false, false);
  return t[0] ^ t[1];
}
__device__ __forceinline__ float frcp(float x) { return __builtin_amdgcn_rcpf(x); }
__device__ __forceinline__ void tile_store(char* d, const f32x4 (&acc)[8], int c) {
#pragma unroll
  for (int i = 0; i < 8; ++i) *(f32x4*)(d + (c * 8 + (i ^ (c & 7))) * 16) = acc[i];
}
__device__ __forceinline__ int tile_off(int col) { return (col ^ ((col >> 3) & 7)) * 16; }

#define STAMP(i) do {} while (0)
#define STAMP_DECL
#define STAMP_FLUSH do {} while (0)

__global__ void __launch_bounds__(NTHR, 1) fused_decode_kernel(Params p) {
  extern __shared__ __attribute__((aligned(16))) char lds[];
  const int tid = threadIdx.x, lane = tid & 63;
  const int wave = __builtin_amdgcn_readfirstlane(tid >> 6);
  const int c = lane & 15, g = lane >> 4;
  const int bid = blockIdx.x, xcd = bid & 7, r = bid >> 3;
  STAMP_DECL
  STAMP(0);

  const int pair = xcd * 2 + (r >> 4), b_att = pair >> 2, kvh = pair & 3, split = r & 15;
  const rsrc_t rs_ws = mk(p.ws, WS_END);
  const rsrc_t rs_x = mk(p.x, NB * DM * 2);
  const rsrc_t rs_wq = mk(p.wq + (size_t)(128 * xcd) * 8192 + r * 256, 128 * 8192 - r * 256);
  const rsrc_t rs_k = mk(p.k + ((size_t)pair * SK + split * 512) * 256, 512 * 256);

  u32x2 e2 = ld_sc64(rs_ws, WS_E, 0);
  u32x4 x0 = ld_pl(rs_x, wave * 2048 + lane * 32, 0);
  u32x4 x1 = ld_pl(rs_x, wave * 2048 + lane * 32 + 16, 0);
  const u32x2 xs = __builtin_amdgcn_raw_buffer_load_b64(rs_x, wave * 2048 + (xcd >> 1) * 512 + lane * 8, 0, 0);
  u32 cw0;
  {
    u64 pc; u32 len;
    asm volatile("s_getpc_b64 %0\n.Lfdk_here:\n\ts_mov_b32 %1, .Lfdk_end-.Lfdk_here" : "=s"(pc), "=s"(len));
    len = (int)len > 0 ? len : 0u;
    const rsrc_t rs_c = mk((const char*)pc, len);
    cw0 = __builtin_amdgcn_raw_buffer_load_b32(rs_c, (wave * 64 + lane) * 128, 0, 0);
  }
  SB();
  u32x4 wq[8], kb[32], vb[4][8], fa[8], fb[8], wo[8], wd[8];
  const u32 vo8 = (u32)(wave * 32 + g * 8) * 8192 + c * 16;
  const u32 vok = (u32)wave * 32768 + lane * 16;
#define ISSUE(n)                                                                                              \
  do {                                                                                                        \
    SB();                                                                                                     \
    _Pragma("unroll") for (int j_ = 0; j_ < 4; ++j_) {                                                        \
      if ((n) < 2) wq[(n) * 4 + j_] = ld_nt(rs_wq, vo8, ((n) * 4 + j_) * 8192);                               \
      else if ((n) < 10) kb[((n) - 2) * 4 + j_] = ld_nt(rs_k, vok, (((n) - 2) * 4 + j_) * 1024);              \
      else if ((n) < 18) vb[((n) - 10) / 2][(((n) - 10) & 1) * 4 + j_] = ld_nt(rs_v, vok, (((n) - 10) * 4 + j_) * 1024); \
      else if ((n) < 20) fa[((n) - 18) * 4 + j_] = ld_nt(rs_wf, vo7, (((n) - 18) * 4 + j_) * 7168);           \
      else if ((n) < 22) fb[((n) - 20) * 4 + j_] = ld_nt(rs_wf, vo7, (128 + ((n) - 20) * 4 + j_) * 7168);     \
      else if ((n) < 24) wo[((n) - 22) * 4 + j_] = ld_nt(rs_wo, vo2, (((n) - 22) * 4 + j_) * 2048);           \
      else wd[((n) - 24) * 4 + j_] = ld_nt(rs_wd, vo2, (((n) - 24) * 4 + j_) * 2048);                         \
    }                                                                                                         \
    SB();                                                                                                     \
  } while (0)
#define ISSUE_A(n)                                                                                            \
  do {                                                                                                        \
    SB();                                                                                                     \
    _Pragma("unroll") for (int j_ = 0; j_ < 4; ++j_) {                                                        \
      if ((n) < 2) wq[(n) * 4 + j_] = ld_nt(rs_wq, vo8, ((n) * 4 + j_) * 8192);                               \
      else kb[((n) - 2) * 4 + j_] = ld_nt(rs_k, vok, (((n) - 2) * 4 + j_) * 1024);                            \
    }                                                                                                         \
    SB();                                                                                                     \
  } while (0)
#define WAITG(n)                                                                                              \
  do {                                                                                                        \
    if ((n) < 2) KEEP(wq[(n) * 4 + 3]);                                                                       \
    else if ((n) < 10) KEEP(kb[((n) - 2) * 4 + 3]);                                                           \
    else if ((n) < 18) KEEP(vb[((n) - 10) / 2][(((n) - 10) & 1) * 4 + 3]);                                    \
    else if ((n) < 20) KEEP(fa[((n) - 18) * 4 + 3]);                                                          \
    else if ((n) < 22) KEEP(fb[((n) - 20) * 4 + 3]);                                                          \
    else if ((n) < 24) KEEP(wo[((n) - 22) * 4 + 3]);                                                          \
    else KEEP(wd[((n) - 24) * 4 + 3]);                                                                        \
  } while (0)
  ISSUE_A(0); ISSUE_A(1); ISSUE_A(2); ISSUE_A(3);

  const bool has_ff = r < 28;
  const int cgf = r + 1 - (r + 1 >= 28 ? 28 : 0);
  const int rgo = xcd * 8 + (r >> 2), cgo = r & 3;
  const int rgd = has_ff ? xcd * 7 + (r >> 2) : 0, cgd = (r + 1) & 3;
  const rsrc_t rs_wsf = mk(p.ws, has_ff ? WS_END : 0u);
  const rsrc_t rs_v = mk(p.v + ((size_t)pair * SK + split * 512) * 256, 512 * 256);
  const rsrc_t rs_wf = mk(((xcd & 1) ? p.wu : p.wg) + (size_t)(256 * (xcd >> 1)) * 7168 + cgf * 256, has_ff ? 256 * 7168 - cgf * 256 : 0);
  const rsrc_t rs_wd = mk(p.wd + (size_t)rgd * 64 * 2048 + cgd * 512, has_ff ? 64 * 2048 - cgd * 512 : 0);
  const rsrc_t rs_wo = mk(p.wo + (size_t)rgo * 64 * 2048 + cgo * 512, 64 * 2048 - cgo * 512);
  const u32 vo7 = (u32)(wave * 32 + g * 8) * 7168 + c * 16;
  const u32 vo2 = (u32)((wave & 1) * 32 + g * 8) * 2048 + (wave >> 1) * 256 + c * 16;

  {
    float v[16];
#pragma unroll
    for (int i = 0; i < 4; ++i) { v[2 * i] = bf_lo(x0[i]); v[2 * i + 1] = bf_hi(x0[i]); v[8 + 2 * i] = bf_lo(x1[i]); v[8 + 2 * i + 1] = bf_hi(x1[i]); }
    float s = 0.f;
#pragma unroll
    for (int i = 0; i < 16; ++i) s += v[i];
    const float mean = wave_sum(s) * (1.f / DM);
    float q = 0.f;
#pragma unroll
    for (int i = 0; i < 16; ++i) { const float d = v[i] - mean; q = fmaf(d, d, q); }
    const float var = wave_sum(q) * (1.f / DM);
    const float mb = rbf(mean), vb_ = rbf(var);
    const float den = rbf(sqrtf(rbf(vb_ + 1e-5f)));
    const u32x2 o2 = {cvt_pk(rbf(bf_lo(xs[0]) - mb) / den, rbf(bf_hi(xs[0]) - mb) / den),
                      cvt_pk(rbf(bf_lo(xs[1]) - mb) / den, rbf(bf_hi(xs[1]) - mb) / den)};
    *(u32x2*)(lds + L_XN + wave * 512 + lane * 8) = o2;
  }
  wg_barrier();
  STAMP(1);
  const u32x4 xfa = *(const u32x4*)(lds + L_XN + (c & 3) * 512 + wave * 64 + g * 16);
  const u32x4 xfb = *(const u32x4*)(lds + L_XN + (c & 3) * 512 + 256 + wave * 64 + g * 16);
  const u64 epoch = (((u64)(u32)__builtin_amdgcn_readfirstlane(e2[1])) << 32 | (u32)__builtin_amdgcn_readfirstlane(e2[0])) + 1ull;
  const u32 T12 = (u32)epoch & 0xfffu;

  WAITG(0); ISSUE(4);
  WAITG(1); ISSUE(5);
  {
    const u32x4 xq = (xcd & 1) ? xfb : xfa;
    f32x4 acc[8];
#pragma unroll
    for (int i = 0; i < 8; ++i) acc[i] = mfma(xq, colfrag(wq, i), f32x4{0.f, 0.f, 0.f, 0.f});
    if (g == 0) {
      char* d = lds + L_REDQ + wave * 2048;
#pragma unroll
      for (int b = 0; b < 4; ++b) {
        *(f32x4*)(d + b * 512 + ((2 * c) ^ (c >> 2 & 1)) * 16) = f32x4{acc[0][b], acc[1][b], acc[2][b], acc[3][b]};
        *(f32x4*)(d + b * 512 + ((2 * c + 1) ^ (c >> 2 & 1)) * 16) = f32x4{acc[4][b], acc[5][b], acc[6][b], acc[7][b]};
      }
    }
  }
  wg_barrier();
  STAMP(2);
  {
    const int t7 = tid & 127;
    const char* s = lds + L_REDQ + (t7 ^ (t7 >> 3 & 1)) * 16;
    const f32x4 a = *(const f32x4*)s + *(const f32x4*)(s + 2048) + *(const f32x4*)(s + 4096) + *(const f32x4*)(s + 6144);
    const u32x4 au = __builtin_bit_cast(u32x4, a);
    const u32 xr = half_xor(au[0] ^ au[1] ^ au[2] ^ au[3]);
    st_sc(au, rs_ws, tid < 128 ? WS_QP + (u32)((xcd * 4 + (t7 >> 5)) * 4096 + r * 128 + (t7 & 31) * 4) * 4 : OOB, 0);
    st_sc(u32x4{(u32)epoch, (u32)(epoch >> 32), xr, 0u}, rs_ws, (tid < 128 && (tid & 31) == 0) ? WS_QC + (u32)(((t7 >> 5) * 32 + r) * 8 + xcd) * 16 : OOB, 0);
  }
  STAMP(3);

  char* kl = lds + L_K + wave * 32768;
#define KSTORE(n)                                                                                   \
  _Pragma("unroll") for (int j = 0; j < 4; ++j) {                                                   \
    const int t_ = ((n) - 2) * 4 + j;                                                               \
    *(u32x4*)(kl + (t_ * 4 + g) * 256 + ((c ^ (g * 4 + j)) * 16)) = kb[t_];                         \
  }
  u32x4 qp[8], qc;
  const u32 qp_off = WS_QP + (u32)(b_att * 4096 + kvh * 1024 + tid * 4) * 4;
  const u32 qc_off = WS_QC + (u32)((b_att * 32 + kvh * 8 + (tid >> 5)) * 8 + (tid & 7)) * 16;
  WAITG(2); ISSUE(6); KSTORE(2)
  WAITG(3); ISSUE(7); KSTORE(3)
  WAITG(4); ISSUE(8); KSTORE(4)
  WAITG(5); ISSUE(9); KSTORE(5)
  STAMP(4);
  WAITG(6); SB(); qp[0] = ld_sc(rs_ws, qp_off, 0); qp[1] = ld_sc(rs_ws, qp_off, 65536); ISSUE(10); KSTORE(6)
  WAITG(7); SB(); qp[2] = ld_sc(rs_ws, qp_off, 2 * 65536); qp[3] = ld_sc(rs_ws, qp_off, 3 * 65536); ISSUE(11); KSTORE(7)
  WAITG(8); SB(); qp[4] = ld_sc(rs_ws, qp_off, 4 * 65536); qp[5] = ld_sc(rs_ws, qp_off, 5 * 65536); ISSUE(12); KSTORE(8)
  WAITG(9); SB(); qp[6] = ld_sc(rs_ws, qp_off, 6 * 65536); qp[7] = ld_sc(rs_ws, qp_off, 7 * 65536); qc = ld_sc(rs_ws, qc_off, 0); ISSUE(13); KSTORE(9)
  STAMP(5);
  WAITG(10); ISSUE(14);
  WAITG(11); ISSUE(15);
  WAITG(12); ISSUE(16);
#define QBAD(bad)                                                                                   \
  {                                                                                                 \
    u32 x_ = (tid & 31) < 8 ? qc[2] : 0u;                                                           \
    _Pragma("unroll") for (int s = 0; s < 8; ++s) x_ ^= qp[s][0] ^ qp[s][1] ^ qp[s][2] ^ qp[s][3];  \
    bad = half_xor(x_) | (qc[0] ^ (u32)epoch) | (qc[1] ^ (u32)(epoch >> 32));                       \
  }
  {
    u32 bad;
    QBAD(bad)
    if (!__all(bad == 0)) {
      for (int spins = 0; spins < SPIN_CAP; ++spins) {
        asm volatile("" ::: "memory");
#pragma unroll
        for (int s = 0; s < 8; ++s) qp[s] = ld_sc(rs_ws, qp_off, s * 65536);
        qc = ld_sc(rs_ws, qc_off, 0);
        QBAD(bad)
        if (__all(bad == 0)) break;
      }
    }
    f32x4 a = __builtin_bit_cast(f32x4, qp[0]);
#pragma unroll
    for (int s = 1; s < 8; ++s) a += __builtin_bit_cast(f32x4, qp[s]);
    *(u32x2*)(lds + L_QS + (tid >> 5) * 272 + (tid & 31) * 8) = u32x2{cvt_pk(a[0], a[1]), cvt_pk(a[2], a[3])};
  }
  wg_barrier();
  STAMP(6);

  u32x4 pf[2];
  float m_w, l_w;
  {
    u32x4 qlo[4], qhi[4];
#pragma unroll
    for (int s = 0; s < 4; ++s) {
      const u32x4 qv = *(const u32x4*)(lds + L_QS + (c & 7) * 272 + (4 * s + g) * 16);
      const u32x4 z = {0u, 0u, 0u, 0u};
      qlo[s] = c < 8 ? qv : z; qhi[s] = c < 8 ? z : qv;
    }
    const int rho = (c & 3) * 4 + (c >> 2);
    float sc[4][4];
#pragma unroll
    for (int u = 0; u < 2; ++u) {
#pragma unroll
      for (int a = 0; a < 2; ++a) {
        f32x4 acc = {0.f, 0.f, 0.f, 0.f};
#pragma unroll
        for (int s = 0; s < 4; ++s) {
          const u32x4 kf = *(const u32x4*)(kl + ((u * 4 + a) * 16 + rho) * 256 + (((4 * s + g) ^ c) * 16));
          acc = mfma(kf, qlo[s], acc);
        }
#pragma unroll
        for (int s = 0; s < 4; ++s) {
          const u32x4 kf = *(const u32x4*)(kl + ((u * 4 + 2 + a) * 16 + rho) * 256 + (((4 * s + g) ^ c) * 16));
          acc = mfma(kf, qhi[s], acc);
        }
        const u32 p01 = cvt_pk(acc[0], acc[1]), p23 = cvt_pk(acc[2], acc[3]);
        const u32 r01 = cvt_pk(bf_lo(p01) * 0.08838834764831845f, bf_hi(p01) * 0.08838834764831845f);
        const u32 r23 = cvt_pk(bf_lo(p23) * 0.08838834764831845f, bf_hi(p23) * 0.08838834764831845f);
        sc[u * 2 + a][0] = bf_lo(r01); sc[u * 2 + a][1] = bf_hi(r01); sc[u * 2 + a][2] = bf_lo(r23); sc[u * 2 + a][3] = bf_hi(r23);
      }
      if (u == 0) { WAITG(13); ISSUE(17); }
    }
    float m = sc[0][0];
#pragma unroll
    for (int a = 0; a < 4; ++a)
#pragma unroll
      for (int i = 0; i < 4; ++i) m = fmaxf(m, sc[a][i]);
    m = fmaxf(m, dpp<XOR8>(m));
    m = xrow_max(m);
    const float ml2 = m * 1.4426950408889634f;
    float l = 0.f;
#pragma unroll
    for (int a = 0; a < 4; ++a) {
#pragma unroll
      for (int i = 0; i < 4; ++i) { sc[a][i] = __builtin_amdgcn_exp2f(fmaf(sc[a][i], 1.4426950408889634f, -ml2)); l += sc[a][i]; }
    }
#pragma unroll
    for (int u = 0; u < 2; ++u) {
      pf[u][0] = cvt_pk(sc[2 * u][0], sc[2 * u][1]);
      pf[u][1] = cvt_pk(sc[2 * u][2], sc[2 * u][3]);
      pf[u][2] = cvt_pk(sc[2 * u + 1][0], sc[2 * u + 1][1]);
      pf[u][3] = cvt_pk(sc[2 * u + 1][2], sc[2 * u + 1][3]);
    }
    l += dpp<XOR8>(l);
    m_w = m; l_w = xrow_sum(l);
  }
  STAMP(7);

  f32x4 oa[8], ob[8];
  WAITG(14); ISSUE(18);
#pragma unroll
  for (int i = 0; i < 8; ++i) oa[i] = mfma(colfrag(vb[0], i), pf[0], f32x4{0.f, 0.f, 0.f, 0.f});
#pragma unroll
  for (int i = 0; i < 8; ++i) ob[i] = mfma(colfrag(vb[1], i), pf[0], f32x4{0.f, 0.f, 0.f, 0.f});
  WAITG(15); ISSUE(19);
#pragma unroll
  for (int i = 0; i < 8; ++i) oa[i] = mfma(colfrag(vb[2], i), pf[1], oa[i]);
  WAITG(16); ISSUE(20);
  WAITG(17); ISSUE(21);
#pragma unroll
  for (int i = 0; i < 8; ++i) ob[i] = mfma(colfrag(vb[3], i), pf[1], ob[i]);
  STAMP(8);
  {
    char* d = kl + LK_MRG + c * 512 + g * 128;
#pragma unroll
    for (int q = 0; q < 4; ++q) {
      f32x4 lo, hi;
#pragma unroll
      for (int i = 0; i < 4; ++i) { lo[i] = c < 8 ? oa[i][q] : ob[i][q]; hi[i] = c < 8 ? oa[4 + i][q] : ob[4 + i][q]; }
      *(f32x4*)(d + ((2 * q) ^ (c & 7)) * 16) = lo;
      *(f32x4*)(d + ((2 * q + 1) ^ (c & 7)) * 16) = hi;
    }
    if (lane < 8) *(float2*)(lds + L_ML + (wave * 8 + c) * 8) = make_float2(m_w, l_w);
  }
  wg_barrier();
  {
    const int hh = tid >> 5, d4 = tid & 31;
    float2 ml[4];
#pragma unroll
    for (int w = 0; w < 4; ++w) ml[w] = *(const float2*)(lds + L_ML + (w * 8 + hh) * 8);
    const float M = fmaxf(fmaxf(ml[0].x, ml[1].x), fmaxf(ml[2].x, ml[3].x));
    f32x4 o = {0.f, 0.f, 0.f, 0.f};
    float L = 0.f;
#pragma unroll
    for (int w = 0; w < 4; ++w) {
      const float f = fexp(ml[w].x - M);
      L = fmaf(f, ml[w].y, L);
      const char* s = lds + L_K + w * 32768 + LK_MRG + hh * 512 + (d4 ^ hh) * 16;
      o += f * (*(const f32x4*)s + *(const f32x4*)(s + 4096));
    }
    const u32 slot = (u32)((b_att * 32 + kvh * 8 + hh) * 16 + split);
    st_sc(tag4(o, T12), rs_ws, WS_OP + slot * 512 + d4 * 16, 0);
    st_sc(tag4(f32x4{M, L, 0.f, 0.f}, T12), rs_ws, d4 == 0 ? WS_ML + slot * 16 : OOB, 0);
  }
  STAMP(9);

  u32x4 og[4], mlv;
  const u32 og_off = WS_OP + (u32)((wave * 32 + (rgo >> 1)) * 16 + g * 4) * 512 + (rgo & 1) * 256 + c * 16;
  const u32 ml_off = WS_ML + (u32)((wave * 32 + (rgo >> 1)) * 16 + ((c + g * 4) & 15)) * 16;
  {
    f32x4 acc[8];
    WAITG(18); ISSUE(22);
    WAITG(19); ISSUE(23);
#pragma unroll
    for (int i = 0; i < 8; ++i) acc[i] = mfma(xfa, colfrag(fa, i), f32x4{0.f, 0.f, 0.f, 0.f});
    WAITG(20); ISSUE(24);
    WAITG(21); SB();
#pragma unroll
    for (int s = 0; s < 4; ++s) og[s] = ld_sc(rs_ws, og_off, s * 512);
    mlv = ld_sc(rs_ws, ml_off, 0);
    ISSUE(25);
#pragma unroll
    for (int i = 0; i < 8; ++i) acc[i] = mfma(xfb, colfrag(fb, i), acc[i]);
    if (g == 0) tile_store(kl + LK_RF, acc, c);
  }
  wg_barrier();
  {
    const char* s = lds + L_K + LK_RF + tile_off(tid & 127);
    f32x4 a = *(const f32x4*)s + *(const f32x4*)(s + 32768) + *(const f32x4*)(s + 65536) + *(const f32x4*)(s + 98304);
    st_sc(tag4(a, T12), rs_wsf, tid < 128 ? WS_GU + (u32)(xcd * DF + cgf * 128 + tid) * 16 : OOB, 0);
  }
  STAMP(10);
  SB();
  u32x4 of_[8];
#pragma unroll
  for (int i = 0; i < 8; ++i) of_[i] = colfrag(wo, i);
  {
    u32 bad = tagbad(mlv, T12);
#pragma unroll
    for (int s = 0; s < 4; ++s) bad |= tagbad(og[s], T12);
    if (!__all(bad == 0)) {
      for (int spins = 0; spins < SPIN_CAP; ++spins) {
        asm volatile("" ::: "memory");
#pragma unroll
        for (int s = 0; s < 4; ++s) og[s] = ld_sc(rs_ws, og_off, s * 512);
        mlv = ld_sc(rs_ws, ml_off, 0);
        bad = tagbad(mlv, T12);
#pragma unroll
        for (int s = 0; s < 4; ++s) bad |= tagbad(og[s], T12);
        if (__all(bad == 0)) break;
      }
    }
  }
  u32x4 gp[4];
  const u32 gu_off = WS_GU + (u32)((lane >> 5) * DF + rgd * 64 + (wave & 1) * 32 + (lane & 31)) * 16;
  asm volatile("" ::: "memory");
#pragma unroll
  for (int s = 0; s < 4; ++s) gp[s] = ld_sc(rs_wsf, gu_off, s * (2 * DF * 16));
  SB();
  {
    const f32x4 mlf = untag(mlv);
    const float M = row16_max(mlf[0]);
    const float w = fexp(mlf[0] - M);
    const float L = row16_sum(w * mlf[1]);
    f32x4 o = dpp<0x150>(w) * untag(og[0]);
    o += dpp<0x151>(w) * untag(og[1]); o += dpp<0x152>(w) * untag(og[2]); o += dpp<0x153>(w) * untag(og[3]);
    const float inv = frcp(L);
#pragma unroll
    for (int i = 0; i < 4; ++i) o[i] = xrow_sum(o[i]) * inv;
    if (lane < 16) *(u32x2*)(lds + L_OS + wave * 128 + lane * 8) = u32x2{cvt_pk(o[0], o[1]), cvt_pk(o[2], o[3])};
  }
  wg_barrier();
  STAMP(11);
  {
    const u32x4 of = *(const u32x4*)(lds + L_OS + (c & 3) * 128 + (wave & 1) * 64 + g * 16);
    f32x4 acc[8];
#pragma unroll
    for (int i = 0; i < 8; ++i) acc[i] = mfma(of, of_[i], f32x4{0.f, 0.f, 0.f, 0.f});
    if (g == 0) tile_store(kl + LK_RO, acc, c);
  }
  wg_barrier();
  {
    const char* s = lds + L_K + (tid >> 7) * 65536 + LK_RO + tile_off(tid & 127);
    f32x4 a = *(const f32x4*)s + *(const f32x4*)(s + 32768);
    st_sc(tag4(a, T12), rs_ws, WS_FP + (u32)(rgo * 1024 + cgo * 256 + tid) * 16, 0);
  }
  STAMP(12);
  SB();
  u32x4 df[8];
#pragma unroll
  for (int i = 0; i < 8; ++i) df[i] = colfrag(wd, i);
  {
    u32 bad = 0;
#pragma unroll
    for (int s = 0; s < 4; ++s) bad |= tagbad(gp[s], T12);
    if (has_ff && !__all(bad == 0)) {
      for (int spins = 0; spins < SPIN_CAP; ++spins) {
        asm volatile("" ::: "memory");
#pragma unroll
        for (int s = 0; s < 4; ++s) gp[s] = ld_sc(rs_wsf, gu_off, s * (2 * DF * 16));
        bad = 0;
#pragma unroll
        for (int s = 0; s < 4; ++s) bad |= tagbad(gp[s], T12);
        if (__all(bad == 0)) break;
      }
    }
    const f32x4 a = (untag(gp[0]) + untag(gp[1])) + (untag(gp[2]) + untag(gp[3]));
#pragma unroll
    for (int b = 0; b < 4; ++b) {
      const float v = rbf(a[b]);
      auto t = __builtin_amdgcn_permlane32_swap(__float_as_uint(v), __float_as_uint(v), false, false);
      const float gv = __uint_as_float(t[0]), uv = __uint_as_float(t[1]);
      const float inner = 0.7978845608028654f * (gv + 0.044715f * gv * gv * gv);
      const float th = 1.f - 2.f * frcp(fexp(2.f * inner) + 1.f);
      const float ge = rbf(0.5f * gv * (1.f + th));
      const u32 hv = cvt_pk(ge * uv, 0.f);
      if (lane < 32) *(unsigned short*)(kl + LK_HS + b * 64 + lane * 2) = (unsigned short)hv;
    }
  }
  asm volatile("s_waitcnt lgkmcnt(0)" ::: "memory");
  STAMP(13);
  {
    const u32x4 hf = *(const u32x4*)(kl + LK_HS + (c & 3) * 64 + g * 16);
    f32x4 acc[8];
#pragma unroll
    for (int i = 0; i < 8; ++i) acc[i] = mfma(hf, df[i], f32x4{0.f, 0.f, 0.f, 0.f});
    if (g == 0) tile_store(kl + LK_RD, acc, c);
  }
  wg_barrier();
  {
    const char* s = lds + L_K + (tid >> 7) * 65536 + LK_RD + tile_off(tid & 127);
    f32x4 a = *(const f32x4*)s + *(const f32x4*)(s + 32768);
    st_sc(tag4(a, T12), rs_wsf, WS_FP + (u32)((64 + rgd) * 1024 + cgd * 256 + tid) * 16, 0);
  }
  STAMP(14);
  SB();
  {
    const int col0 = (xcd * 32 + r) * 4;
    const int ks = tid >> 2, cc = tid & 3;
    const bool act = ks < 56;
    const u32 off_o = WS_FP + (u32)(ks * 1024 + col0 + cc) * 16;
    const u32 off_d = act ? off_o + 64 * 1024 * 16 : OOB;
    const unsigned short xin = *(const unsigned short*)(p.x + (((tid >> 2) & 3) * DM + col0 + cc) * 2);
    u32x4 fo, fd;
    for (int spins = 0; spins < SPIN_CAP; ++spins) {
      fo = ld_sc(rs_ws, off_o, 0);
      fd = ld_sc(rs_ws, off_d, 0);
      const u32 bad = tagbad(fo, T12) | (act ? tagbad(fd, T12) : 0u);
      if (__all(bad == 0)) break;
      asm volatile("" ::: "memory");
    }
    f32x4 a = untag(fo), d = untag(fd);
#pragma unroll
    for (int i = 0; i < 4; ++i) {
      float t = a[i], u = d[i];
      t += dpp<0x124>(t); u += dpp<0x124>(u);
      t += dpp<XOR8>(t); u += dpp<XOR8>(u);
      a[i] = xrow_sum(t); d[i] = xrow_sum(u);
    }
    if (lane < 4) {
      *(f32x4*)(lds + L_K + LK_FIN + (wave * 4 + lane) * 16) = a;
      *(f32x4*)(lds + L_K + LK_FIN + 256 + (wave * 4 + lane) * 16) = d;
    }
    wg_barrier();
    if (tid < 16) {
      const int b = tid >> 2, cc2 = tid & 3;
      const float* fs = (const float*)(lds + L_K + LK_FIN) + cc2 * 4 + b;
      const float s = rbf((fs[0] + fs[16]) + (fs[32] + fs[48]));
      const float dd = rbf((fs[64] + fs[80]) + (fs[96] + fs[112]));
      const float gt = rbf(dd * frcp(1.f + fexp(-dd)));
      const float o = bf_lo(xin) + rbf(s * gt);
      *(unsigned short*)(p.out + (b * DM + col0 + cc2) * 2) = (unsigned short)cvt_pk(o, o);
    }
    if (bid == 0 && tid == 0) st_sc64(u32x2{(u32)epoch, (u32)(epoch >> 32)}, rs_ws, WS_E, 0);
  }
  KEEP(cw0);
  asm volatile(".Lfdk_end:");
  STAMP(15);
  STAMP_FLUSH;
}

extern "C" void kernel_launch(void* const* d_in, const int* in_sizes, int n_in, void* d_out, int out_size, void* d_ws,
                              size_t ws_size, hipStream_t stream) {
  Params p;
  p.x = (const char*)d_in[0]; p.k = (const char*)d_in[1]; p.v = (const char*)d_in[2];
  p.wq = (const char*)d_in[3]; p.wo = (const char*)d_in[4];
  p.wg = (const char*)d_in[6]; p.wu = (const char*)d_in[7]; p.wd = (const char*)d_in[8];
  p.out = (char*)d_out; p.ws = (char*)d_ws;
  (void)hipFuncSetAttribute((const void*)fused_decode_kernel, hipFuncAttributeMaxDynamicSharedMemorySize, L_END);
  fused_decode_kernel<<<NWG, NTHR, L_END, stream>>>(p);
}
```

```cpp
#include <hip/hip_runtime.h>
#include <stdint.h>

typedef unsigned u32;
typedef unsigned long long u64;
typedef u32 u32x4 __attribute__((ext_vector_type(4)));
typedef u32 u32x2 __attribute__((ext_vector_type(2)));
typedef float f32x4 __attribute__((ext_vector_type(4)));
typedef __bf16 bf16x8 __attribute__((ext_vector_type(8)));
typedef __amdgpu_buffer_rsrc_t rsrc_t;

constexpr int NB = 4, DM = 1024, HQ = 32, HKV = 4, SK = 8192, DH = 128, DF = 3584;
constexpr int NWG = 256, NTHR = 256;

constexpr u32 WS_E = 0;
constexpr u32 WS_CF = 1024;
constexpr u32 WS_QC = 4096;
constexpr u32 WS_QP = 32768;
constexpr u32 WS_GU = WS_QP + 8 * 4 * 4096 * 4;
constexpr u32 WS_OP = WS_GU + 4 * 2 * DF * 16;
constexpr u32 WS_ML = WS_OP + 4 * 32 * 16 * 256;
constexpr u32 WS_FP = WS_ML + 4 * 32 * 16 * 16;
constexpr u32 WS_END = WS_FP + 60 * 1024 * 16;

constexpr int L_K = 0;
constexpr int L_REDQ = 131072;
constexpr int L_XN = L_REDQ + 8192;
constexpr int L_QS = L_XN + 4 * 2064;
constexpr int L_OS = L_QS + 8 * 272;
constexpr int L_ML = L_OS + 1024;
constexpr int L_CL = L_ML + 256;
constexpr int L_END = L_CL + 16;
constexpr int LK_MRG = 0;
constexpr int LK_RF = 8192;
constexpr int LK_RD = 10240;
constexpr int LK_RO = 12288;
constexpr int LK_HS = 14336;
constexpr int LK_FIN = 16384;

struct Params {
  const char* x; const char* k; const char* v; const char* wq; const char* wo;
  const char* wg; const char* wu; const char* wd; char* out; char* ws;
};

#define SB() __builtin_amdgcn_sched_barrier(0)
#define KEEP(x) do { SB(); asm volatile("" ::"v"(x)); } while (0)

__device__ __forceinline__ rsrc_t mk(const char* p, u32 bytes) {
  return __builtin_amdgcn_make_buffer_rsrc((void*)p, 0, bytes, 0x00020000);
}
__device__ __forceinline__ u32x4 ld_nt(rsrc_t r, u32 voff, u32 soff) { return __builtin_amdgcn_raw_buffer_load_b128(r, voff, soff, 2); }
__device__ __forceinline__ u32x4 ld_pl(rsrc_t r, u32 voff, u32 soff) { return __builtin_amdgcn_raw_buffer_load_b128(r, voff, soff, 0); }
__device__ __forceinline__ u32x4 ld_sc(rsrc_t r, u32 voff, u32 soff) { return __builtin_amdgcn_raw_buffer_load_b128(r, voff, soff, 16); }
__device__ __forceinline__ u32x2 ld_sc64(rsrc_t r, u32 voff, u32 soff) { return __builtin_amdgcn_raw_buffer_load_b64(r, voff, soff, 16); }
__device__ __forceinline__ void st_sc(u32x4 v, rsrc_t r, u32 voff, u32 soff) { __builtin_amdgcn_raw_buffer_store_b128(v, r, voff, soff, 16); }
__device__ __forceinline__ void st_sc64(u32x2 v, rsrc_t r, u32 voff, u32 soff) { __builtin_amdgcn_raw_buffer_store_b64(v, r, voff, soff, 16); }

__device__ __forceinline__ void wg_barrier() {
  asm volatile("s_waitcnt lgkmcnt(0)\n\ts_barrier" ::: "memory");
}
__device__ __forceinline__ float bf_lo(u32 v) { return __uint_as_float(v << 16); }
__device__ __forceinline__ float bf_hi(u32 v) { return __uint_as_float(v & 0xffff0000u); }
typedef __bf16 bf16x2 __attribute__((ext_vector_type(2)));
typedef float f32x2 __attribute__((ext_vector_type(2)));
__device__ __forceinline__ u32 cvt_pk(float lo, float hi) {
  return __builtin_bit_cast(u32, __builtin_convertvector(f32x2{lo, hi}, bf16x2));
}
__device__ __forceinline__ float rbf(float f) { return bf_lo(cvt_pk(f, f)); }

template <int CTRL> __device__ __forceinline__ float dpp(float x) {
  return __builtin_bit_cast(float, __builtin_amdgcn_mov_dpp(__builtin_bit_cast(int, x), CTRL, 0xf, 0xf, true));
}
constexpr int XOR1 = 0xB1, XOR2 = 0x4E, XOR7 = 0x141, XOR8 = 0x128;
__device__ __forceinline__ float xrow_sum(float x) {
  auto s = __builtin_amdgcn_permlane16_swap(__float_as_uint(x), __float_as_uint(x), false, false);
  x = __uint_as_float(s[0]) + __uint_as_float(s[1]);
  auto t = __builtin_amdgcn_permlane32_swap(__float_as_uint(x), __float_as_uint(x), false, false);
  return __uint_as_float(t[0]) + __uint_as_float(t[1]);
}
__device__ __forceinline__ float xrow_max(float x) {
  auto s = __builtin_amdgcn_permlane16_swap(__float_as_uint(x), __float_as_uint(x), false, false);
  x = fmaxf(__uint_as_float(s[0]), __uint_as_float(s[1]));
  auto t = __builtin_amdgcn_permlane32_swap(__float_as_uint(x), __float_as_uint(x), false, false);
  return fmaxf(__uint_as_float(t[0]), __uint_as_float(t[1]));
}
__device__ __forceinline__ float x32_sum(float x) {
  auto t = __builtin_amdgcn_permlane32_swap(__float_as_uint(x), __float_as_uint(x), false, false);
  return __uint_as_float(t[0]) + __uint_as_float(t[1]);
}
__device__ __forceinline__ float wave_sum(float x) {
  x += dpp<XOR1>(x); x += dpp<XOR2>(x); x += dpp<XOR7>(x); x += dpp<XOR8>(x);
  return xrow_sum(x);
}
__device__ __forceinline__ float row16_max(float x) {
  x = fmaxf(x, dpp<XOR1>(x)); x = fmaxf(x, dpp<XOR2>(x)); x = fmaxf(x, dpp<XOR7>(x)); x = fmaxf(x, dpp<XOR8>(x));
  return x;
}
__device__ __forceinline__ float row16_sum(float x) {
  x += dpp<XOR1>(x); x += dpp<XOR2>(x); x += dpp<XOR7>(x); x += dpp<XOR8>(x);
  return x;
}
__device__ __forceinline__ float fexp(float x) { return __builtin_amdgcn_exp2f(x * 1.4426950408889634f); }

__device__ __forceinline__ f32x4 mfma(u32x4 a, u32x4 b, f32x4 c) {
  return __builtin_amdgcn_mfma_f32_16x16x32_bf16(__builtin_bit_cast(bf16x8, a), __builtin_bit_cast(bf16x8, b), c, 0, 0, 0);
}
__device__ __forceinline__ u32x4 colfrag(const u32x4 (&w)[8], int i) {
  const u32 sel = (i & 1) ? 0x07060302u : 0x05040100u;
  const int d = i >> 1;
  u32x4 f;
  f[0] = __builtin_amdgcn_perm(w[1][d], w[0][d], sel);
  f[1] = __builtin_amdgcn_perm(w[3][d], w[2][d], sel);
  f[2] = __builtin_amdgcn_perm(w[5][d], w[4][d], sel);
  f[3] = __builtin_amdgcn_perm(w[7][d], w[6][d], sel);
  return f;
}
__device__ __forceinline__ u32 tag1(float v, u32 t) { return ((__float_as_uint(v) + 0x800u) & 0xfffff000u) | t; }
__device__ __forceinline__ u32x4 tag4(f32x4 v, u32 t) { u32x4 r = {tag1(v[0], t), tag1(v[1], t), tag1(v[2], t), tag1(v[3], t)}; return r; }
__device__ __forceinline__ u32 tagbad(u32x4 v, u32 t) { return ((v[0] ^ t) | (v[1] ^ t) | (v[2] ^ t) | (v[3] ^ t)) & 0xfffu; }
__device__ __forceinline__ f32x4 untag(u32x4 v) {
  f32x4 r = {__uint_as_float(v[0] & 0xfffff000u), __uint_as_float(v[1] & 0xfffff000u), __uint_as_float(v[2] & 0xfffff000u), __uint_as_float(v[3] & 0xfffff000u)};
  return r;
}
constexpr int SPIN_CAP = 1 << 18;
#define DIST 4
#define NSH 4
#define CLPOS 14
#define GORD 2
#define LATE21 2
#define PRE_Q 1
#define PRE_O 0
__device__ __forceinline__ u32 at_add(u32 v, rsrc_t r, u32 voff) {
  return (u32)__builtin_amdgcn_raw_ptr_buffer_atomic_add_i32((int)v, r, voff, 0, 0);
}
constexpr u32 OOB = 0x80000000u;
template <int CTRL> __device__ __forceinline__ u32 dppu(u32 x) { return (u32)__builtin_amdgcn_mov_dpp((int)x, CTRL, 0xf, 0xf, true); }
__device__ __forceinline__ u32 half_xor(u32 x) {
  x ^= dppu<XOR1>(x); x ^= dppu<XOR2>(x); x ^= dppu<XOR7>(x); x ^= dppu<XOR8>(x);
  auto t = __builtin_amdgcn_permlane16_swap(x, x, false, false);
  return t[0] ^ t[1];
}
__device__ __forceinline__ u32 row_xor(u32 x) {
  x ^= dppu<XOR1>(x); x ^= dppu<XOR2>(x); x ^= dppu<XOR7>(x); x ^= dppu<XOR8>(x);
  return x;
}
__device__ __forceinline__ float frcp(float x) { return __builtin_amdgcn_rcpf(x); }
__device__ __forceinline__ void tile_store(char* d, const f32x4 (&acc)[8], int c) {
#pragma unroll
  for (int i = 0; i < 8; ++i) *(f32x4*)(d + (c * 8 + (i ^ (c & 7))) * 16) = acc[i];
}
__device__ __forceinline__ void amask(u32x4 a, int c, u32x4 (&am)[4]) {
  const u32x4 z = {0u, 0u, 0u, 0u};
#pragma unroll
  for (int q = 0; q < 4; ++q) am[q] = (c >> 2) == q ? a : z;
}
__device__ __forceinline__ void tile_store2(char* d, const f32x4 (&acc)[2], int c, int g) {
#pragma unroll
  for (int j = 0; j < 2; ++j) *(f32x4*)(d + (c * 8 + ((j * 4 + g) ^ (c & 7))) * 16) = acc[j];
}
__device__ __forceinline__ int tile_off(int col) { return (col ^ ((col >> 3) & 7)) * 16; }

#define STAMP(i) do {} while (0)
#define STAMP_DECL
#define STAMP_FLUSH do {} while (0)

__global__ void __launch_bounds__(NTHR, 1) fused_decode_kernel(Params p) {
  extern __shared__ __attribute__((aligned(16))) char lds[];
  const int tid = threadIdx.x, lane = tid & 63;
  const int wave = __builtin_amdgcn_readfirstlane(tid >> 6);
  const int c = lane & 15, g = lane >> 4;
  const int bid = blockIdx.x, xcd = bid & 7, r = bid >> 3;
  STAMP_DECL
  STAMP(0);

  const int pair = xcd * 2 + (r >> 4), b_att = pair >> 2, kvh = pair & 3, split = r & 15;
  const rsrc_t rs_ws = mk(p.ws, WS_END);
  const rsrc_t rs_x = mk(p.x, NB * DM * 2);
  const rsrc_t rs_wq = mk(p.wq + (size_t)(128 * xcd) * 8192 + r * 256, 128 * 8192 - r * 256);
  const rsrc_t rs_k = mk(p.k + ((size_t)pair * SK + split * 512) * 256, 512 * 256);

  u32x2 e2 = ld_sc64(rs_ws, WS_E, 0);
  u32x4 x0 = ld_pl(rs_x, wave * 2048 + lane * 32, 0);
  u32x4 x1 = ld_pl(rs_x, wave * 2048 + lane * 32 + 16, 0);
  u32 cw0;
  {
    u64 pc; u32 len;
    asm volatile("s_getpc_b64 %0\n.Lfdk_here:\n\ts_mov_b32 %1, .Lfdk_end-.Lfdk_here" : "=s"(pc), "=s"(len));
    len = (int)len > 0 ? len : 0u;
    const rsrc_t rs_c = mk((const char*)pc, len);
    cw0 = __builtin_amdgcn_raw_buffer_load_b32(rs_c, (wave * 64 + lane) * 128, 0, 0);
  }
  SB();
  u32x4 wq[8], kb[32], vb[4][8], fa[8], fb[8], wo[8], wd[8];
  const u32 vo8 = (u32)(wave * 32 + g * 8) * 8192 + c * 16;
  const u32 vok = (u32)wave * 1024 + lane * 16;
#define ISSUE(n)                                                                                              \
  do {                                                                                                        \
    SB();                                                                                                     \
    _Pragma("unroll") for (int j_ = 0; j_ < 4; ++j_) {                                                        \
      if ((n) < 2) wq[(n) * 4 + j_] = ld_nt(rs_wq, vo8, ((n) * 4 + j_) * 8192);                               \
      else if ((n) < 10) kb[((n) - 2) * 4 + j_] = ld_nt(rs_k, vok, (((n) - 2) * 4 + j_) * 4096);              \
      else if ((n) < 18) vb[((n) - 10) / 2][(((n) - 10) & 1) * 4 + j_] = ld_nt(rs_v, vok, (((n) - 10) * 4 + j_) * 4096); \
      else if ((n) < 20) fa[((n) - 18) * 4 + j_] = ld_nt(rs_wf, vo7, (((n) - 18) * 4 + j_) * 7168);           \
      else if ((n) < 22) fb[((n) - 20) * 4 + j_] = ld_nt(rs_wf, vo7, (128 + ((n) - 20) * 4 + j_) * 7168);     \
      else if ((n) < 24) wo[((n) - 22) * 4 + j_] = ld_nt(rs_wo, vo2o, (j_ * 8 + ((n) - 22)) * 2048);          \
      else wd[((n) - 24) * 4 + j_] = ld_nt(rs_wd, vo2, (((n) - 24) * 4 + j_) * 2048);                         \
    }                                                                                                         \
    SB();                                                                                                     \
  } while (0)
#define ISSUE_A(n)                                                                                            \
  do {                                                                                                        \
    SB();                                                                                                     \
    _Pragma("unroll") for (int j_ = 0; j_ < 4; ++j_) {                                                        \
      if ((n) < 2) wq[(n) * 4 + j_] = ld_nt(rs_wq, vo8, ((n) * 4 + j_) * 8192);                               \
      else kb[((n) - 2) * 4 + j_] = ld_nt(rs_k, vok, (((n) - 2) * 4 + j_) * 4096);                            \
    }                                                                                                         \
    SB();                                                                                                     \
  } while (0)
#define WAITG(n)                                                                                              \
  do {                                                                                                        \
    if ((n) < 2) KEEP(wq[(n) * 4 + 3]);                                                                       \
    else if ((n) < 10) KEEP(kb[((n) - 2) * 4 + 3]);                                                           \
    else if ((n) < 18) KEEP(vb[((n) - 10) / 2][(((n) - 10) & 1) * 4 + 3]);                                    \
    else if ((n) < 20) KEEP(fa[((n) - 18) * 4 + 3]);                                                          \
    else if ((n) < 22) KEEP(fb[((n) - 20) * 4 + 3]);                                                          \
    else if ((n) < 24) KEEP(wo[((n) - 22) * 4 + 3]);                                                          \
    else KEEP(wd[((n) - 24) * 4 + 3]);                                                                        \
  } while (0)
#define SCHED 2, 3, 4, 4, 5, 6, 7, 8, 9, 10, 11, 13, 14, 15, 15, 16, 17, 18, 20, 20, 21, 22, 23, 24, 25
  constexpr int NXT[25] = {SCHED};
#define UPTO(n)                                              \
  do {                                                       \
    if (NXT[n] + 1 <= NXT[(n) + 1]) ISSUE(NXT[n] + 1);       \
    if (NXT[n] + 2 <= NXT[(n) + 1]) ISSUE(NXT[n] + 2);       \
    if (NXT[n] + 3 <= NXT[(n) + 1]) ISSUE(NXT[n] + 3);       \
  } while (0)
  ISSUE_A(0); ISSUE_A(1);
  if (NXT[0] >= 2) ISSUE_A(2);
  if (NXT[0] >= 3) ISSUE_A(3);

  const int rgo = xcd * 4 + (r >> 3), cgo = r & 7;
  const rsrc_t rs_v = mk(p.v + ((size_t)pair * SK + split * 512) * 256, 512 * 256);
  const rsrc_t rs_wo = mk(p.wo + (size_t)rgo * 128 * 2048 + cgo * 256, 128 * 2048 - cgo * 256);
  rsrc_t rs_wf = rs_x, rs_wd = rs_x;
  const u32 vo7 = (u32)(wave * 32 + g * 8) * 7168 + c * 16;
  const u32 vo2 = (u32)(wave * 32 + g * 8) * 2048 + c * 16;
  const u32 vo2o = (u32)(wave * 32 + g * 2) * 2048 + c * 16;
  const int shard = ((r >> 2) + r + xcd) & (NSH - 1);

  {
    float v[16];
#pragma unroll
    for (int i = 0; i < 4; ++i) { v[2 * i] = bf_lo(x0[i]); v[2 * i + 1] = bf_hi(x0[i]); v[8 + 2 * i] = bf_lo(x1[i]); v[8 + 2 * i + 1] = bf_hi(x1[i]); }
    float s = 0.f;
#pragma unroll
    for (int i = 0; i < 16; ++i) s += v[i];
    const float mean = wave_sum(s) * (1.f / DM);
    float q = 0.f;
#pragma unroll
    for (int i = 0; i < 16; ++i) { const float d = v[i] - mean; q = fmaf(d, d, q); }
    const float var = wave_sum(q) * (1.f / DM);
    const float mb = rbf(mean), vb_ = rbf(var);
    const float den = rbf(sqrtf(rbf(vb_ + 1e-5f)));
    const float rd = frcp(den);
    u32x4 o0, o1;
#pragma unroll
    for (int i = 0; i < 8; ++i) {
      const float a0 = rbf(v[2 * i] - mb), a1 = rbf(v[2 * i + 1] - mb);
      float q0 = a0 * rd, q1 = a1 * rd;
      q0 = fmaf(fmaf(-q0, den, a0), rd, q0); q1 = fmaf(fmaf(-q1, den, a1), rd, q1);
      if (i < 4) o0[i] = cvt_pk(q0, q1); else o1[i - 4] = cvt_pk(q0, q1);
    }
    char* d = lds + L_XN + wave * 2064 + lane * 32;
    *(u32x4*)d = o0; *(u32x4*)(d + 16) = o1;
  }
  wg_barrier();
  STAMP(1);
  const u32x4 xq = *(const u32x4*)(lds + L_XN + (c & 3) * 2064 + xcd * 256 + wave * 64 + g * 16);
  const u64 epoch = (((u64)(u32)__builtin_amdgcn_readfirstlane(e2[1])) << 32 | (u32)__builtin_amdgcn_readfirstlane(e2[0])) + 1ull;
  const u32 T12 = (u32)epoch & 0xfffu;
  const u32 QTAG = ((u32)epoch & 0xfffffu) << 12;
  __builtin_amdgcn_raw_buffer_store_b32(QTAG, rs_ws, (bid == 0 && tid < NSH) ? WS_CF + tid * 256 : OOB, 0, 16);

  WAITG(0); UPTO(0);
  WAITG(1); UPTO(1);
  {
    f32x4 acc[8];
#pragma unroll
    for (int i = 0; i < 8; ++i) acc[i] = mfma(xq, colfrag(wq, i), f32x4{0.f, 0.f, 0.f, 0.f});
    if (g == 0) {
      char* d = lds + L_REDQ + wave * 2048;
#pragma unroll
      for (int b = 0; b < 4; ++b) {
        *(f32x4*)(d + b * 512 + ((2 * c) ^ (c >> 2 & 1)) * 16) = f32x4{acc[0][b], acc[1][b], acc[2][b], acc[3][b]};
        *(f32x4*)(d + b * 512 + ((2 * c + 1) ^ (c >> 2 & 1)) * 16) = f32x4{acc[4][b], acc[5][b], acc[6][b], acc[7][b]};
      }
    }
  }
  wg_barrier();
  STAMP(2);
  {
    const int t7 = tid & 127;
    const char* s = lds + L_REDQ + (t7 ^ (t7 >> 3 & 1)) * 16;
    const f32x4 a = *(const f32x4*)s + *(const f32x4*)(s + 2048) + *(const f32x4*)(s + 4096) + *(const f32x4*)(s + 6144);
    const u32x4 au = __builtin_bit_cast(u32x4, a);
    const u32 xr = half_xor(au[0] ^ au[1] ^ au[2] ^ au[3]);
    st_sc(au, rs_ws, tid < 128 ? WS_QP + (u32)((xcd * 4 + (t7 >> 5)) * 4096 + r * 128 + (t7 & 31) * 4) * 4 : OOB, 0);
    st_sc(u32x4{(u32)epoch, (u32)(epoch >> 32), xr, 0u}, rs_ws, (tid < 128 && (tid & 31) == 0) ? WS_QC + (u32)(((t7 >> 5) * 32 + r) * 8 + xcd) * 16 : OOB, 0);
  }
  STAMP(3);

  char* kl = lds + L_K + wave * 32768;
#define KSTORE(n)                                                                                   \
  _Pragma("unroll") for (int j = 0; j < 4; ++j) {                                                   \
    const int t_ = ((n) - 2) * 4 + j;                                                               \
    *(u32x4*)(kl + (t_ * 4 + g) * 256 + ((c ^ (g * 4 + j)) * 16)) = kb[t_];                         \
  }
  u32x4 qp[8], qc;
  const u32 qp_off = WS_QP + (u32)(b_att * 4096 + kvh * 1024 + tid * 4) * 4;
  const u32 qc_off = WS_QC + (u32)((b_att * 32 + kvh * 8 + (tid >> 5)) * 8 + (tid & 7)) * 16;
  WAITG(2); UPTO(2); KSTORE(2)
  WAITG(3); UPTO(3); KSTORE(3)
  WAITG(4); UPTO(4); KSTORE(4)
  WAITG(5); UPTO(5); KSTORE(5)
  STAMP(4);
  WAITG(6); SB(); qp[0] = ld_sc(rs_ws, qp_off, 0); qp[1] = ld_sc(rs_ws, qp_off, 65536); UPTO(6); KSTORE(6)
  WAITG(7); SB(); qp[2] = ld_sc(rs_ws, qp_off, 2 * 65536); qp[3] = ld_sc(rs_ws, qp_off, 3 * 65536); UPTO(7); KSTORE(7)
  WAITG(8); SB(); qp[4] = ld_sc(rs_ws, qp_off, 4 * 65536); qp[5] = ld_sc(rs_ws, qp_off, 5 * 65536); UPTO(8); KSTORE(8)
  WAITG(9); SB(); qp[6] = ld_sc(rs_ws, qp_off, 6 * 65536); qp[7] = ld_sc(rs_ws, qp_off, 7 * 65536); qc = ld_sc(rs_ws, qc_off, 0); UPTO(9); KSTORE(9)
  STAMP(5);
  WAITG(10); UPTO(10);
#define QBAD(bad)                                                                                   \
  {                                                                                                 \
    u32 x_ = (tid & 31) < 8 ? qc[2] : 0u;                                                           \
    _Pragma("unroll") for (int s = 0; s < 8; ++s) x_ ^= qp[s][0] ^ qp[s][1] ^ qp[s][2] ^ qp[s][3];  \
    bad = half_xor(x_) | (qc[0] ^ (u32)epoch) | (qc[1] ^ (u32)(epoch >> 32));                       \
  }
  {
    u32 bad;
    QBAD(bad)
    if (!__all(bad == 0)) {
      for (int spins = 0; spins < SPIN_CAP; ++spins) {
        asm volatile("" ::: "memory");
#pragma unroll
        for (int s = 0; s < 8; ++s) qp[s] = ld_sc(rs_ws, qp_off, s * 65536);
        qc = ld_sc(rs_ws, qc_off, 0);
        QBAD(bad)
        if (__all(bad == 0)) break;
      }
    }
    f32x4 a = __builtin_bit_cast(f32x4, qp[0]);
#pragma unroll
    for (int s = 1; s < 8; ++s) a += __builtin_bit_cast(f32x4, qp[s]);
    *(u32x2*)(lds + L_QS + (tid >> 5) * 272 + (tid & 31) * 8) = u32x2{cvt_pk(a[0], a[1]), cvt_pk(a[2], a[3])};
  }
  wg_barrier();
  STAMP(6);

  u32x4 pf[2];
  float m_w, l_w;
  {
    u32x4 qlo[4], qhi[4];
#pragma unroll
    for (int s = 0; s < 4; ++s) {
      const u32x4 qv = *(const u32x4*)(lds + L_QS + (c & 7) * 272 + (4 * s + g) * 16);
      const u32x4 z = {0u, 0u, 0u, 0u};
      qlo[s] = c < 8 ? qv : z; qhi[s] = c < 8 ? z : qv;
    }
    const int rho = (c & 3) * 4 + (c >> 2);
    float sc[4][4];
#pragma unroll
    for (int u = 0; u < 2; ++u) {
#pragma unroll
      for (int a = 0; a < 2; ++a) {
        f32x4 acc = {0.f, 0.f, 0.f, 0.f};
#pragma unroll
        for (int s = 0; s < 4; ++s) {
          const u32x4 kf = *(const u32x4*)(kl + ((u * 4 + a) * 16 + rho) * 256 + (((4 * s + g) ^ c) * 16));
          acc = mfma(kf, qlo[s], acc);
        }
#pragma unroll
        for (int s = 0; s < 4; ++s) {
          const u32x4 kf = *(const u32x4*)(kl + ((u * 4 + 2 + a) * 16 + rho) * 256 + (((4 * s + g) ^ c) * 16));
          acc = mfma(kf, qhi[s], acc);
        }
        const u32 p01 = cvt_pk(acc[0], acc[1]), p23 = cvt_pk(acc[2], acc[3]);
        const u32 r01 = cvt_pk(bf_lo(p01) * 0.08838834764831845f, bf_hi(p01) * 0.08838834764831845f);
        const u32 r23 = cvt_pk(bf_lo(p23) * 0.08838834764831845f, bf_hi(p23) * 0.08838834764831845f);
        sc[u * 2 + a][0] = bf_lo(r01); sc[u * 2 + a][1] = bf_hi(r01); sc[u * 2 + a][2] = bf_lo(r23); sc[u * 2 + a][3] = bf_hi(r23);
      }
      if (u == 0) { WAITG(11); UPTO(11); }
    }
    float m = sc[0][0];
#pragma unroll
    for (int a = 0; a < 4; ++a)
#pragma unroll
      for (int i = 0; i < 4; ++i) m = fmaxf(m, sc[a][i]);
    m = fmaxf(m, dpp<XOR8>(m));
    m = xrow_max(m);
    const float ml2 = m * 1.4426950408889634f;
    float l = 0.f;
#pragma unroll
    for (int a = 0; a < 4; ++a) {
#pragma unroll
      for (int i = 0; i < 4; ++i) { sc[a][i] = __builtin_amdgcn_exp2f(fmaf(sc[a][i], 1.4426950408889634f, -ml2)); l += sc[a][i]; }
    }
#pragma unroll
    for (int u = 0; u < 2; ++u) {
      pf[u][0] = cvt_pk(sc[2 * u][0], sc[2 * u][1]);
      pf[u][1] = cvt_pk(sc[2 * u][2], sc[2 * u][3]);
      pf[u][2] = cvt_pk(sc[2 * u + 1][0], sc[2 * u + 1][1]);
      pf[u][3] = cvt_pk(sc[2 * u + 1][2], sc[2 * u + 1][3]);
    }
    l += dpp<XOR8>(l);
    m_w = m; l_w = xrow_sum(l);
  }
  STAMP(7);

  f32x4 oa[8];
  u32x4 pl[2], ph[2];
  {
    const u32x4 z = {0u, 0u, 0u, 0u};
#pragma unroll
    for (int u = 0; u < 2; ++u) { pl[u] = c < 8 ? pf[u] : z; ph[u] = c < 8 ? z : pf[u]; }
  }
  WAITG(12); UPTO(12);
#pragma unroll
  for (int i = 0; i < 8; ++i) oa[i] = mfma(colfrag(vb[0], i), pl[0], f32x4{0.f, 0.f, 0.f, 0.f});
  WAITG(13); SB();
  u32 clm = 0;
  if (CLPOS == 13) clm = at_add(1u, rs_ws, tid == 0 ? WS_CF + shard * 256 : OOB);
  UPTO(13);
#pragma unroll
  for (int i = 0; i < 8; ++i) oa[i] = mfma(colfrag(vb[1], i), ph[0], oa[i]);
  WAITG(14); SB();
  if (CLPOS == 14) clm = at_add(1u, rs_ws, tid == 0 ? WS_CF + shard * 256 : OOB);
  UPTO(14);
  WAITG(15); UPTO(15);
#pragma unroll
  for (int i = 0; i < 8; ++i) oa[i] = mfma(colfrag(vb[2], i), pl[1], oa[i]);
  WAITG(16);
  if (tid == 0 && (clm & 0xfffff000u) != QTAG) {
    for (int spins = 0; (clm & 0xfffff000u) != QTAG && spins < SPIN_CAP; ++spins) clm = at_add(1u, rs_ws, WS_CF + shard * 256);
  }
  if (tid == 0) *(u32*)(lds + L_CL) = clm & 0xfffu;
  wg_barrier();
  const int kq = __builtin_amdgcn_readfirstlane(*(const u32*)(lds + L_CL));
  constexpr int PF = 224 / NSH, PS = 32 / NSH;
  const bool has_f = kq < PF, has_d = kq < PF - PS || kq >= PF;
  const int tfc = has_f ? shard * PF + kq : 0;
  const int kgf = tfc / 28, cgf = tfc - kgf * 28;
  rs_wf = mk(((kgf & 1) ? p.wu : p.wg) + (size_t)(256 * (kgf >> 1)) * 7168 + cgf * 256, has_f ? 256 * 7168 - cgf * 256 : 0);
  const int tdn = shard * PF + (kq >= PF ? kq - PF : (has_d ? kq + PS : 0));
  const int rgd = tdn >> 3, cgd = tdn & 7;
  rs_wd = mk(p.wd + (size_t)rgd * 128 * 2048 + cgd * 256, has_d ? 128 * 2048 - cgd * 256 : 0);
  const rsrc_t rs_wsd = mk(p.ws, has_d ? WS_END : 0u);
  UPTO(16);
  WAITG(17); UPTO(17);
#pragma unroll
  for (int i = 0; i < 8; ++i) oa[i] = mfma(colfrag(vb[3], i), ph[1], oa[i]);
  STAMP(8);
  {
    char* d = kl + LK_MRG + c * 512 + g * 128;
#pragma unroll
    for (int i = 0; i < 8; ++i) *(f32x4*)(d + (i ^ (c & 7)) * 16) = oa[i];
    if (lane < 8) *(float2*)(lds + L_ML + (wave * 8 + c) * 8) = make_float2(m_w, l_w);
  }
  wg_barrier();
  {
    const int hh = tid >> 5, d4 = tid & 31;
    float2 ml[4];
#pragma unroll
    for (int w = 0; w < 4; ++w) ml[w] = *(const float2*)(lds + L_ML + (w * 8 + hh) * 8);
    const float M = fmaxf(fmaxf(ml[0].x, ml[1].x), fmaxf(ml[2].x, ml[3].x));
    f32x4 o = {0.f, 0.f, 0.f, 0.f};
    float L = 0.f;
#pragma unroll
    for (int w = 0; w < 4; ++w) {
      const float f = fexp(ml[w].x - M);
      L = fmaf(f, ml[w].y, L);
      const char* s = lds + L_K + w * 32768 + LK_MRG + hh * 512 + (d4 ^ hh) * 16;
      o += f * (*(const f32x4*)s + *(const f32x4*)(s + 4096));
    }
    o *= frcp(L);
    const u32 p0 = cvt_pk(o[0], o[1]), p1 = cvt_pk(o[2], o[3]);
    const u32x4 pk = {p0, p1, dppu<XOR1>(p0), dppu<XOR1>(p1)};
    const u32 xr = half_xor(p0 ^ p1) ^ __float_as_uint(M) ^ __float_as_uint(L) ^ (u32)(epoch >> 32);
    const u32 slot = (u32)((b_att * 32 + kvh * 8 + hh) * 16 + split);
    st_sc(pk, rs_ws, (d4 & 1) == 0 ? WS_OP + slot * 256 + d4 * 8 : OOB, 0);
    st_sc(u32x4{__float_as_uint(M), __float_as_uint(L), xr, (u32)epoch}, rs_ws, d4 == 0 ? WS_ML + slot * 16 : OOB, 0);
  }
  STAMP(9);

  u32x4 og[4], mlv;
  const u32 og_off = WS_OP + (u32)((wave * 32 + rgo) * 16 + g * 4) * 256 + c * 16;
  const u32 ml_off = WS_ML + (u32)((wave * 32 + rgo) * 16 + ((c + g * 4) & 15)) * 16;
  {
    f32x4 acc[2] = {{0.f, 0.f, 0.f, 0.f}, {0.f, 0.f, 0.f, 0.f}};
    const u32x4 xfa = *(const u32x4*)(lds + L_XN + (c & 3) * 2064 + (kgf >> 1) * 512 + wave * 64 + g * 16);
    const u32x4 xfb = *(const u32x4*)(lds + L_XN + (c & 3) * 2064 + (kgf >> 1) * 512 + 256 + wave * 64 + g * 16);
    u32x4 ama[4], amb[4];
    amask(xfa, c, ama); amask(xfb, c, amb);
    WAITG(18); UPTO(18);
    WAITG(19); UPTO(19);
#pragma unroll
    for (int i = 0; i < 8; ++i) acc[i >> 2] = mfma(ama[i & 3], colfrag(fa, i), acc[i >> 2]);
    WAITG(20); UPTO(20);
    WAITG(21); SB();
#define OG25() do { SB(); _Pragma("unroll") for (int s = 0; s < 4; ++s) og[s] = ld_sc(rs_ws, og_off, s * 256); mlv = ld_sc(rs_ws, ml_off, 0); UPTO(21); } while (0)
    if (GORD == 0) OG25();
#pragma unroll
    for (int i = 0; i < 8; ++i) acc[i >> 2] = mfma(amb[i & 3], colfrag(fb, i), acc[i >> 2]);
    tile_store2(kl + LK_RF, acc, c, g);
    if (GORD == 1) OG25();
  }
  wg_barrier();
  {
    const char* s = lds + L_K + LK_RF + tile_off(tid & 127);
    f32x4 a = (*(const f32x4*)s + *(const f32x4*)(s + 32768)) + (*(const f32x4*)(s + 65536) + *(const f32x4*)(s + 98304));
    st_sc(tag4(a, T12), rs_ws, (has_f && tid < 128) ? WS_GU + (u32)(kgf * DF + cgf * 128 + tid) * 16 : OOB, 0);
  }
  if (GORD == 2) OG25();
  STAMP(10);
  SB();
  WAITG(22); UPTO(22);
#define OBAD(bad)                                                                                                  \
  {                                                                                                                \
    const u32 want = mlv[2] ^ mlv[0] ^ mlv[1] ^ (u32)(epoch >> 32);              \
    u32 x_ = c < 4 ? want : 0u;                                         \
    _Pragma("unroll") for (int s = 0; s < 4; ++s) x_ ^= og[s][0] ^ og[s][1] ^ og[s][2] ^ og[s][3];                 \
    bad = row_xor(x_) | (mlv[3] ^ (u32)epoch);                                                                     \
  }
  {
    u32 bad;
    OBAD(bad)
    if (!__all(bad == 0)) {
      for (int spins = 0; spins < SPIN_CAP; ++spins) {
        asm volatile("" ::: "memory");
#pragma unroll
        for (int s = 0; s < 4; ++s) og[s] = ld_sc(rs_ws, og_off, s * 256);
        mlv = ld_sc(rs_ws, ml_off, 0);
        OBAD(bad)
        if (__all(bad == 0)) break;
      }
    }
  }
  {
    const float ms = __uint_as_float(mlv[0]), ls = __uint_as_float(mlv[1]);
    const float M = row16_max(ms);
    const float wl = fexp(ms - M) * ls;
    const float w = wl * frcp(row16_sum(wl));
    const float w0 = dpp<0x150>(w), w1 = dpp<0x151>(w), w2 = dpp<0x152>(w), w3 = dpp<0x153>(w);
    u32x4 pk;
#pragma unroll
    for (int i = 0; i < 4; ++i) {
      float lo = w0 * bf_lo(og[0][i]), hi = w0 * bf_hi(og[0][i]);
      lo = fmaf(w1, bf_lo(og[1][i]), lo); hi = fmaf(w1, bf_hi(og[1][i]), hi);
      lo = fmaf(w2, bf_lo(og[2][i]), lo); hi = fmaf(w2, bf_hi(og[2][i]), hi);
      lo = fmaf(w3, bf_lo(og[3][i]), lo); hi = fmaf(w3, bf_hi(og[3][i]), hi);
      pk[i] = cvt_pk(xrow_sum(lo), xrow_sum(hi));
    }
    if (lane < 16) *(u32x4*)(lds + L_OS + wave * 256 + lane * 16) = pk;
  }
  WAITG(23);
  u32x4 gp[4];
  const u32 gu_off = WS_GU + (u32)((lane >> 5) * DF + rgd * 128 + wave * 32 + (lane & 31)) * 16;
  asm volatile("" ::: "memory");
#pragma unroll
  for (int s = 0; s < 4; ++s) gp[s] = ld_sc(rs_wsd, gu_off, s * (2 * DF * 16));
  SB();
  UPTO(23);
  u32x4 of_[8];
#pragma unroll
  for (int i = 0; i < 8; ++i) of_[i] = colfrag(wo, i);
  wg_barrier();
  STAMP(11);
  {
    const u32x4 of = *(const u32x4*)(lds + L_OS + (c & 3) * 256 + wave * 64 + g * 16);
    f32x4 acc[2] = {{0.f, 0.f, 0.f, 0.f}, {0.f, 0.f, 0.f, 0.f}};
    u32x4 am[4];
    amask(of, c, am);
#pragma unroll
    for (int i = 0; i < 8; ++i) acc[i & 1] = mfma(am[i >> 1], of_[(i & 1) * 4 + (i >> 1)], acc[i & 1]);
    tile_store2(kl + LK_RO, acc, c, g);
  }
  wg_barrier();
  {
    const char* s = lds + L_K + LK_RO + tile_off(tid & 127);
    f32x4 a = (*(const f32x4*)s + *(const f32x4*)(s + 32768)) + (*(const f32x4*)(s + 65536) + *(const f32x4*)(s + 98304));
    st_sc(tag4(a, T12), rs_ws, tid < 128 ? WS_FP + (u32)(rgo * 1024 + cgo * 128 + tid) * 16 : OOB, 0);
  }
  STAMP(12);
  SB();
  {
    u32 bad = 0;
#pragma unroll
    for (int s = 0; s < 4; ++s) bad |= tagbad(gp[s], T12);
    if (has_d && !__all(bad == 0)) {
      for (int spins = 0; spins < SPIN_CAP; ++spins) {
        asm volatile("" ::: "memory");
#pragma unroll
        for (int s = 0; s < 4; ++s) gp[s] = ld_sc(rs_wsd, gu_off, s * (2 * DF * 16));
        bad = 0;
#pragma unroll
        for (int s = 0; s < 4; ++s) bad |= tagbad(gp[s], T12);
        if (__all(bad == 0)) break;
      }
    }
    const f32x4 a = (untag(gp[0]) + untag(gp[1])) + (untag(gp[2]) + untag(gp[3]));
#pragma unroll
    for (int b = 0; b < 4; ++b) {
      const float v = rbf(a[b]);
      auto t = __builtin_amdgcn_permlane32_swap(__float_as_uint(v), __float_as_uint(v), false, false);
      const float gv = __uint_as_float(t[0]), uv = __uint_as_float(t[1]);
      const float inner = 0.7978845608028654f * (gv + 0.044715f * gv * gv * gv);
      const float th = 1.f - 2.f * frcp(fexp(2.f * inner) + 1.f);
      const float ge = rbf(0.5f * gv * (1.f + th));
      const u32 hv = cvt_pk(ge * uv, 0.f);
      if (lane < 32) *(unsigned short*)(kl + LK_HS + b * 64 + lane * 2) = (unsigned short)hv;
    }
  }
  asm volatile("s_waitcnt lgkmcnt(0)" ::: "memory");
  STAMP(13);
  SB();
  u32x4 df[8];
#pragma unroll
  for (int i = 0; i < 8; ++i) df[i] = colfrag(wd, i);
  {
    const u32x4 hf = *(const u32x4*)(kl + LK_HS + (c & 3) * 64 + g * 16);
    f32x4 acc[2] = {{0.f, 0.f, 0.f, 0.f}, {0.f, 0.f, 0.f, 0.f}};
    u32x4 am[4];
    amask(hf, c, am);
#pragma unroll
    for (int i = 0; i < 8; ++i) acc[i & 1] = mfma(am[i >> 1], df[(i & 1) * 4 + (i >> 1)], acc[i & 1]);
    tile_store2(kl + LK_RD, acc, c, g);
  }
  wg_barrier();
  {
    const char* s = lds + L_K + LK_RD + tile_off(tid & 127);
    f32x4 a = (*(const f32x4*)s + *(const f32x4*)(s + 32768)) + (*(const f32x4*)(s + 65536) + *(const f32x4*)(s + 98304));
    st_sc(tag4(a, T12), rs_wsd, tid < 128 ? WS_FP + (u32)((32 + rgd) * 1024 + cgd * 128 + tid) * 16 : OOB, 0);
  }
  STAMP(14);
  SB();
  {
    const int col0 = (xcd * 32 + r) * 4;
    const int ks = tid >> 2, cc = tid & 3;
    const bool act = ks < 60;
    const u32 off = act ? WS_FP + (u32)(ks * 1024 + col0 + cc) * 16 : OOB;
    const unsigned short xin = *(const unsigned short*)(p.x + (((tid >> 2) & 3) * DM + col0 + cc) * 2);
    u32x4 fv;
    for (int spins = 0; spins < SPIN_CAP; ++spins) {
      fv = ld_sc(rs_ws, off, 0);
      const u32 bad = act ? tagbad(fv, T12) : 0u;
      if (__all(bad == 0)) break;
      asm volatile("" ::: "memory");
    }
    f32x4 a = untag(fv);
#pragma unroll
    for (int i = 0; i < 4; ++i) {
      float t = a[i];
      t += dpp<0x124>(t);
      t += dpp<XOR8>(t);
      a[i] = xrow_sum(t);
    }
    if (lane < 4) *(f32x4*)(lds + L_K + LK_FIN + (wave * 4 + lane) * 16) = a;
    wg_barrier();
    if (tid < 16) {
      const int b = tid >> 2, cc2 = tid & 3;
      const float* fs = (const float*)(lds + L_K + LK_FIN) + cc2 * 4 + b;
      const float s = rbf(fs[0] + fs[16]);
      const float dd = rbf(fs[32] + fs[48]);
      const float gt = rbf(dd * frcp(1.f + fexp(-dd)));
      const float o = bf_lo(xin) + rbf(s * gt);
      *(unsigned short*)(p.out + (b * DM + col0 + cc2) * 2) = (unsigned short)cvt_pk(o, o);
    }
    if (bid == 0 && tid == 0) st_sc64(u32x2{(u32)epoch, (u32)(epoch >> 32)}, rs_ws, WS_E, 0);
  }
  KEEP(cw0);
  asm volatile(".Lfdk_end:");
  STAMP(15);
  STAMP_FLUSH;
}

extern "C" void kernel_launch(void* const* d_in, const int* in_sizes, int n_in, void* d_out, int out_size, void* d_ws,
                              size_t ws_size, hipStream_t stream) {
  Params p;
  p.x = (const char*)d_in[0]; p.k = (const char*)d_in[1]; p.v = (const char*)d_in[2];
  p.wq = (const char*)d_in[3]; p.wo = (const char*)d_in[4];
  p.wg = (const char*)d_in[6]; p.wu = (const char*)d_in[7]; p.wd = (const char*)d_in[8];
  p.out = (char*)d_out; p.ws = (char*)d_ws;
  (void)hipFuncSetAttribute((const void*)fused_decode_kernel, hipFuncAttributeMaxDynamicSharedMemorySize, L_END);
  fused_decode_kernel<<<NWG, NTHR, L_END, stream>>>(p);
}
```

```cpp
#include <hip/hip_runtime.h>
#include <stdint.h>

typedef unsigned u32;
typedef unsigned long long u64;
typedef u32 u32x4 __attribute__((ext_vector_type(4)));
typedef u32 u32x2 __attribute__((ext_vector_type(2)));
typedef float f32x4 __attribute__((ext_vector_type(4)));
typedef __bf16 bf16x8 __attribute__((ext_vector_type(8)));
typedef __amdgpu_buffer_rsrc_t rsrc_t;

constexpr int NB = 4, DM = 1024, SK = 8192, DF = 3584;
constexpr int NWG = 256, NTHR = 256;

constexpr u32 WS_E = 0;
constexpr u32 WS_CF = 1024;
constexpr u32 WS_QC = 4096;
constexpr u32 WS_QP = 32768;
constexpr u32 WS_GU = WS_QP + 8 * 4 * 4096 * 4;
constexpr u32 WS_OP = WS_GU + 4 * 2 * DF * 16;
constexpr u32 WS_ML = WS_OP + 4 * 32 * 16 * 256;
constexpr u32 WS_FP = WS_ML + 4 * 32 * 16 * 16;
constexpr u32 WS_END = WS_FP + 60 * 1024 * 16;

constexpr int L_K = 0;
constexpr int L_REDQ = 131072;
constexpr int L_XN = L_REDQ + 8192;
constexpr int L_QS = L_XN + 4 * 2064;
constexpr int L_OS = L_QS + 8 * 272;
constexpr int L_ML = L_OS + 1024;
constexpr int L_CL = L_ML + 256;
constexpr int L_END = L_CL + 16;
constexpr int LK_MRG = 0;
constexpr int LK_RF = 8192;
constexpr int LK_RD = 10240;
constexpr int LK_RO = 12288;
constexpr int LK_HS = 14336;
constexpr int LK_FIN = 16384;

struct Params {
  const char* x; const char* k; const char* v; const char* wq; const char* wo;
  const char* wg; const char* wu; const char* wd; char* out; char* ws;
};

#define SB() __builtin_amdgcn_sched_barrier(0)
#define KEEP(x) do { SB(); asm volatile("" ::"v"(x)); } while (0)

__device__ __forceinline__ rsrc_t mk(const char* p, u32 bytes) {
  return __builtin_amdgcn_make_buffer_rsrc((void*)p, 0, bytes, 0x00020000);
}
__device__ __forceinline__ u32x4 ld_nt(rsrc_t r, u32 voff, u32 soff) { return __builtin_amdgcn_raw_buffer_load_b128(r, voff, soff, 2); }
__device__ __forceinline__ u32x4 ld_pl(rsrc_t r, u32 voff, u32 soff) { return __builtin_amdgcn_raw_buffer_load_b128(r, voff, soff, 0); }
__device__ __forceinline__ u32x4 ld_sc(rsrc_t r, u32 voff, u32 soff) { return __builtin_amdgcn_raw_buffer_load_b128(r, voff, soff, 16); }
__device__ __forceinline__ u32x2 ld_sc64(rsrc_t r, u32 voff, u32 soff) { return __builtin_amdgcn_raw_buffer_load_b64(r, voff, soff, 16); }
__device__ __forceinline__ void st_sc(u32x4 v, rsrc_t r, u32 voff, u32 soff) { __builtin_amdgcn_raw_buffer_store_b128(v, r, voff, soff, 16); }
__device__ __forceinline__ void st_sc64(u32x2 v, rsrc_t r, u32 voff, u32 soff) { __builtin_amdgcn_raw_buffer_store_b64(v, r, voff, soff, 16); }

__device__ __forceinline__ void wg_barrier() {
  asm volatile("s_waitcnt lgkmcnt(0)\n\ts_barrier" ::: "memory");
}
__device__ __forceinline__ float bf_lo(u32 v) { return __uint_as_float(v << 16); }
__device__ __forceinline__ float bf_hi(u32 v) { return __uint_as_float(v & 0xffff0000u); }
typedef __bf16 bf16x2 __attribute__((ext_vector_type(2)));
typedef float f32x2 __attribute__((ext_vector_type(2)));
__device__ __forceinline__ u32 cvt_pk(float lo, float hi) {
  return __builtin_bit_cast(u32, __builtin_convertvector(f32x2{lo, hi}, bf16x2));
}
__device__ __forceinline__ float rbf(float f) { return bf_lo(cvt_pk(f, f)); }

template <int CTRL> __device__ __forceinline__ float dpp(float x) {
  return __builtin_bit_cast(float, __builtin_amdgcn_mov_dpp(__builtin_bit_cast(int, x), CTRL, 0xf, 0xf, true));
}
constexpr int XOR1 = 0xB1, XOR2 = 0x4E, XOR7 = 0x141, XOR8 = 0x128;
__device__ __forceinline__ float xrow_sum(float x) {
  auto s = __builtin_amdgcn_permlane16_swap(__float_as_uint(x), __float_as_uint(x), false, false);
  x = __uint_as_float(s[0]) + __uint_as_float(s[1]);
  auto t = __builtin_amdgcn_permlane32_swap(__float_as_uint(x), __float_as_uint(x), false, false);
  return __uint_as_float(t[0]) + __uint_as_float(t[1]);
}
__device__ __forceinline__ float xrow_max(float x) {
  auto s = __builtin_amdgcn_permlane16_swap(__float_as_uint(x), __float_as_uint(x), false, false);
  x = fmaxf(__uint_as_float(s[0]), __uint_as_float(s[1]));
  auto t = __builtin_amdgcn_permlane32_swap(__float_as_uint(x), __float_as_uint(x), false, false);
  return fmaxf(__uint_as_float(t[0]), __uint_as_float(t[1]));
}
__device__ __forceinline__ float wave_sum(float x) {
  x += dpp<XOR1>(x); x += dpp<XOR2>(x); x += dpp<XOR7>(x); x += dpp<XOR8>(x);
  return xrow_sum(x);
}
__device__ __forceinline__ float row16_max(float x) {
  x = fmaxf(x, dpp<XOR1>(x)); x = fmaxf(x, dpp<XOR2>(x)); x = fmaxf(x, dpp<XOR7>(x)); x = fmaxf(x, dpp<XOR8>(x));
  return x;
}
__device__ __forceinline__ float row16_sum(float x) {
  x += dpp<XOR1>(x); x += dpp<XOR2>(x); x += dpp<XOR7>(x); x += dpp<XOR8>(x);
  return x;
}
__device__ __forceinline__ float fexp(float x) { return __builtin_amdgcn_exp2f(x * 1.4426950408889634f); }

__device__ __forceinline__ f32x4 mfma(u32x4 a, u32x4 b, f32x4 c) {
  return __builtin_amdgcn_mfma_f32_16x16x32_bf16(__builtin_bit_cast(bf16x8, a), __builtin_bit_cast(bf16x8, b), c, 0, 0, 0);
}
__device__ __forceinline__ u32x4 colfrag(const u32x4 (&w)[8], int i) {
  const u32 sel = (i & 1) ? 0x07060302u : 0x05040100u;
  const int d = i >> 1;
  u32x4 f;
  f[0] = __builtin_amdgcn_perm(w[1][d], w[0][d], sel);
  f[1] = __builtin_amdgcn_perm(w[3][d], w[2][d], sel);
  f[2] = __builtin_amdgcn_perm(w[5][d], w[4][d], sel);
  f[3] = __builtin_amdgcn_perm(w[7][d], w[6][d], sel);
  return f;
}
__device__ __forceinline__ u32 tag1(float v, u32 t) { return ((__float_as_uint(v) + 0x800u) & 0xfffff000u) | t; }
__device__ __forceinline__ u32x4 tag4(f32x4 v, u32 t) { u32x4 r = {tag1(v[0], t), tag1(v[1], t), tag1(v[2], t), tag1(v[3], t)}; return r; }
__device__ __forceinline__ u32 tagbad(u32x4 v, u32 t) { return ((v[0] ^ t) | (v[1] ^ t) | (v[2] ^ t) | (v[3] ^ t)) & 0xfffu; }
__device__ __forceinline__ f32x4 untag(u32x4 v) {
  f32x4 r = {__uint_as_float(v[0] & 0xfffff000u), __uint_as_float(v[1] & 0xfffff000u), __uint_as_float(v[2] & 0xfffff000u), __uint_as_float(v[3] & 0xfffff000u)};
  return r;
}
constexpr int SPIN_CAP = 1 << 18;
constexpr int NSH = 4;
__device__ __forceinline__ u32 at_add(u32 v, rsrc_t r, u32 voff) {
  return (u32)__builtin_amdgcn_raw_ptr_buffer_atomic_add_i32((int)v, r, voff, 0, 0);
}
constexpr u32 OOB = 0x80000000u;
template <int CTRL> __device__ __forceinline__ u32 dppu(u32 x) { return (u32)__builtin_amdgcn_mov_dpp((int)x, CTRL, 0xf, 0xf, true); }
__device__ __forceinline__ u32 half_xor(u32 x) {
  x ^= dppu<XOR1>(x); x ^= dppu<XOR2>(x); x ^= dppu<XOR7>(x); x ^= dppu<XOR8>(x);
  auto t = __builtin_amdgcn_permlane16_swap(x, x, false, false);
  return t[0] ^ t[1];
}
__device__ __forceinline__ u32 row_xor(u32 x) {
  x ^= dppu<XOR1>(x); x ^= dppu<XOR2>(x); x ^= dppu<XOR7>(x); x ^= dppu<XOR8>(x);
  return x;
}
__device__ __forceinline__ float frcp(float x) { return __builtin_amdgcn_rcpf(x); }
__device__ __forceinline__ void amask(u32x4 a, int c, u32x4 (&am)[4]) {
  const u32x4 z = {0u, 0u, 0u, 0u};
#pragma unroll
  for (int q = 0; q < 4; ++q) am[q] = (c >> 2) == q ? a : z;
}
__device__ __forceinline__ void tile_store2(char* d, const f32x4 (&acc)[2], int c, int g) {
#pragma unroll
  for (int j = 0; j < 2; ++j) *(f32x4*)(d + (c * 8 + ((j * 4 + g) ^ (c & 7))) * 16) = acc[j];
}
__device__ __forceinline__ int tile_off(int col) { return (col ^ ((col >> 3) & 7)) * 16; }

__global__ void __launch_bounds__(NTHR, 1) fused_decode_kernel(Params p) {
  extern __shared__ __attribute__((aligned(16))) char lds[];
  const int tid = threadIdx.x, lane = tid & 63;
  const int wave = __builtin_amdgcn_readfirstlane(tid >> 6);
  const int c = lane & 15, g = lane >> 4;
  const int bid = blockIdx.x, xcd = bid & 7, r = bid >> 3;

  const int pair = xcd * 2 + (r >> 4), b_att = pair >> 2, kvh = pair & 3, split = r & 15;
  const rsrc_t rs_ws = mk(p.ws, WS_END);
  const rsrc_t rs_x = mk(p.x, NB * DM * 2);
  const rsrc_t rs_wq = mk(p.wq + (size_t)(128 * xcd) * 8192 + r * 256, 128 * 8192 - r * 256);
  const rsrc_t rs_k = mk(p.k + ((size_t)pair * SK + split * 512) * 256, 512 * 256);

  u32x2 e2 = ld_sc64(rs_ws, WS_E, 0);
  u32x4 x0 = ld_pl(rs_x, wave * 2048 + lane * 32, 0);
  u32x4 x1 = ld_pl(rs_x, wave * 2048 + lane * 32 + 16, 0);
  u32 cw0;
  {
    u64 pc; u32 len;
    asm volatile("s_getpc_b64 %0\n.Lfdk_here:\n\ts_mov_b32 %1, .Lfdk_end-.Lfdk_here" : "=s"(pc), "=s"(len));
    len = (int)len > 0 ? len : 0u;
    const rsrc_t rs_c = mk((const char*)pc, len);
    cw0 = __builtin_amdgcn_raw_buffer_load_b32(rs_c, (wave * 64 + lane) * 128, 0, 0);
  }
  SB();
  u32x4 wq[8], kb[32], vb[4][8], fa[8], fb[8], wo[8], wd[8];
  const u32 vo8 = (u32)(wave * 32 + g * 8) * 8192 + c * 16;
  const u32 vok = (u32)wave * 1024 + lane * 16;
#define ISSUE(n)                                                                                              \
  do {                                                                                                        \
    SB();                                                                                                     \
    _Pragma("unroll") for (int j_ = 0; j_ < 4; ++j_) {                                                        \
      if ((n) < 2) wq[(n) * 4 + j_] = ld_nt(rs_wq, vo8, ((n) * 4 + j_) * 8192);                               \
      else if ((n) < 10) kb[((n) - 2) * 4 + j_] = ld_nt(rs_k, vok, (((n) - 2) * 4 + j_) * 4096);              \
      else if ((n) < 18) vb[((n) - 10) / 2][(((n) - 10) & 1) * 4 + j_] = ld_nt(rs_v, vok, (((n) - 10) * 4 + j_) * 4096); \
      else if ((n) < 20) fa[((n) - 18) * 4 + j_] = ld_nt(rs_wf, vo7, (((n) - 18) * 4 + j_) * 7168);           \
      else if ((n) < 22) fb[((n) - 20) * 4 + j_] = ld_nt(rs_wf, vo7, (128 + ((n) - 20) * 4 + j_) * 7168);     \
      else if ((n) < 24) wo[((n) - 22) * 4 + j_] = ld_nt(rs_wo, vo2o, (j_ * 8 + ((n) - 22)) * 2048);          \
      else wd[((n) - 24) * 4 + j_] = ld_nt(rs_wd, vo2, (((n) - 24) * 4 + j_) * 2048);                         \
    }                                                                                                         \
    SB();                                                                                                     \
  } while (0)
#define ISSUE_A(n)                                                                                            \
  do {                                                                                                        \
    SB();                                                                                                     \
    _Pragma("unroll") for (int j_ = 0; j_ < 4; ++j_) {                                                        \
      if ((n) < 2) wq[(n) * 4 + j_] = ld_nt(rs_wq, vo8, ((n) * 4 + j_) * 8192);                               \
      else kb[((n) - 2) * 4 + j_] = ld_nt(rs_k, vok, (((n) - 2) * 4 + j_) * 4096);                            \
    }                                                                                                         \
    SB();                                                                                                     \
  } while (0)
#define WAITG(n)                                                                                              \
  do {                                                                                                        \
    if ((n) < 2) KEEP(wq[(n) * 4 + 3]);                                                                       \
    else if ((n) < 10) KEEP(kb[((n) - 2) * 4 + 3]);                                                           \
    else if ((n) < 18) KEEP(vb[((n) - 10) / 2][(((n) - 10) & 1) * 4 + 3]);                                    \
    else if ((n) < 20) KEEP(fa[((n) - 18) * 4 + 3]);                                                          \
    else if ((n) < 22) KEEP(fb[((n) - 20) * 4 + 3]);                                                          \
    else if ((n) < 24) KEEP(wo[((n) - 22) * 4 + 3]);                                                          \
    else KEEP(wd[((n) - 24) * 4 + 3]);                                                                        \
  } while (0)
  constexpr int NXT[25] = {2, 3, 4, 4, 5, 6, 7, 8, 9, 10, 11, 13, 14, 15, 15, 16, 17, 18, 19, 20, 21, 22, 23, 24, 25};
#define UPTO(n)                                              \
  do {                                                       \
    if (NXT[n] + 1 <= NXT[(n) + 1]) ISSUE(NXT[n] + 1);       \
    if (NXT[n] + 2 <= NXT[(n) + 1]) ISSUE(NXT[n] + 2);       \
    if (NXT[n] + 3 <= NXT[(n) + 1]) ISSUE(NXT[n] + 3);       \
  } while (0)
  ISSUE_A(0); ISSUE_A(1); ISSUE_A(2);

  const int rgo = xcd * 4 + (r >> 3), cgo = r & 7;
  const rsrc_t rs_v = mk(p.v + ((size_t)pair * SK + split * 512) * 256, 512 * 256);
  const rsrc_t rs_wo = mk(p.wo + (size_t)rgo * 128 * 2048 + cgo * 256, 128 * 2048 - cgo * 256);
  rsrc_t rs_wf = rs_x, rs_wd = rs_x;
  const u32 vo7 = (u32)(wave * 32 + g * 8) * 7168 + c * 16;
  const u32 vo2 = (u32)(wave * 32 + g * 8) * 2048 + c * 16;
  const u32 vo2o = (u32)(wave * 32 + g * 2) * 2048 + c * 16;
  const int shard = ((r >> 2) + r + xcd) & (NSH - 1);

  {
    float v[16];
#pragma unroll
    for (int i = 0; i < 4; ++i) { v[2 * i] = bf_lo(x0[i]); v[2 * i + 1] = bf_hi(x0[i]); v[8 + 2 * i] = bf_lo(x1[i]); v[8 + 2 * i + 1] = bf_hi(x1[i]); }
    float s = 0.f;
#pragma unroll
    for (int i = 0; i < 16; ++i) s += v[i];
    const float mean = wave_sum(s) * (1.f / DM);
    float q = 0.f;
#pragma unroll
    for (int i = 0; i < 16; ++i) { const float d = v[i] - mean; q = fmaf(d, d, q); }
    const float var = wave_sum(q) * (1.f / DM);
    const float mb = rbf(mean), vb_ = rbf(var);
    const float den = rbf(sqrtf(rbf(vb_ + 1e-5f)));
    const float rd = frcp(den);
    u32x4 o0, o1;
#pragma unroll
    for (int i = 0; i < 8; ++i) {
      const float a0 = rbf(v[2 * i] - mb), a1 = rbf(v[2 * i + 1] - mb);
      float q0 = a0 * rd, q1 = a1 * rd;
      q0 = fmaf(fmaf(-q0, den, a0), rd, q0); q1 = fmaf(fmaf(-q1, den, a1), rd, q1);
      if (i < 4) o0[i] = cvt_pk(q0, q1); else o1[i - 4] = cvt_pk(q0, q1);
    }
    char* d = lds + L_XN + wave * 2064 + lane * 32;
    *(u32x4*)d = o0; *(u32x4*)(d + 16) = o1;
  }
  wg_barrier();
  const u32x4 xq = *(const u32x4*)(lds + L_XN + (c & 3) * 2064 + xcd * 256 + wave * 64 + g * 16);
  const u64 epoch = (((u64)(u32)__builtin_amdgcn_readfirstlane(e2[1])) << 32 | (u32)__builtin_amdgcn_readfirstlane(e2[0])) + 1ull;
  const u32 T12 = (u32)epoch & 0xfffu;
  const u32 QTAG = ((u32)epoch & 0xfffffu) << 12;
  __builtin_amdgcn_raw_buffer_store_b32(QTAG, rs_ws, (bid == 0 && tid < NSH) ? WS_CF + tid * 256 : OOB, 0, 16);

  WAITG(0); UPTO(0);
  WAITG(1); UPTO(1);
  {
    f32x4 acc[8];
#pragma unroll
    for (int i = 0; i < 8; ++i) acc[i] = mfma(xq, colfrag(wq, i), f32x4{0.f, 0.f, 0.f, 0.f});
    if (g == 0) {
      char* d = lds + L_REDQ + wave * 2048;
#pragma unroll
      for (int b = 0; b < 4; ++b) {
        *(f32x4*)(d + b * 512 + ((2 * c) ^ (c >> 2 & 1)) * 16) = f32x4{acc[0][b], acc[1][b], acc[2][b], acc[3][b]};
        *(f32x4*)(d + b * 512 + ((2 * c + 1) ^ (c >> 2 & 1)) * 16) = f32x4{acc[4][b], acc[5][b], acc[6][b], acc[7][b]};
      }
    }
  }
  wg_barrier();
  {
    const int t7 = tid & 127;
    const char* s = lds + L_REDQ + (t7 ^ (t7 >> 3 & 1)) * 16;
    const f32x4 a = *(const f32x4*)s + *(const f32x4*)(s + 2048) + *(const f32x4*)(s + 4096) + *(const f32x4*)(s + 6144);
    const u32x4 au = __builtin_bit_cast(u32x4, a);
    const u32 xr = half_xor(au[0] ^ au[1] ^ au[2] ^ au[3]);
    st_sc(au, rs_ws, tid < 128 ? WS_QP + (u32)((xcd * 4 + (t7 >> 5)) * 4096 + r * 128 + (t7 & 31) * 4) * 4 : OOB, 0);
    st_sc(u32x4{(u32)epoch, (u32)(epoch >> 32), xr, 0u}, rs_ws, (tid < 128 && (tid & 31) == 0) ? WS_QC + (u32)(((t7 >> 5) * 32 + r) * 8 + xcd) * 16 : OOB, 0);
  }

  char* kl = lds + L_K + wave * 32768;
#define KSTORE(n)                                                                                   \
  _Pragma("unroll") for (int j = 0; j < 4; ++j) {                                                   \
    const int t_ = ((n) - 2) * 4 + j;                                                               \
    *(u32x4*)(kl + (t_ * 4 + g) * 256 + ((c ^ (g * 4 + j)) * 16)) = kb[t_];                         \
  }
  u32x4 qp[8], qc;
  const u32 qp_off = WS_QP + (u32)(b_att * 4096 + kvh * 1024 + tid * 4) * 4;
  const u32 qc_off = WS_QC + (u32)((b_att * 32 + kvh * 8 + (tid >> 5)) * 8 + (tid & 7)) * 16;
  WAITG(2); UPTO(2); KSTORE(2)
  WAITG(3); UPTO(3); KSTORE(3)
  WAITG(4); UPTO(4); KSTORE(4)
  WAITG(5); UPTO(5); KSTORE(5)
  WAITG(6); SB(); qp[0] = ld_sc(rs_ws, qp_off, 0); qp[1] = ld_sc(rs_ws, qp_off, 65536); UPTO(6); KSTORE(6)
  WAITG(7); SB(); qp[2] = ld_sc(rs_ws, qp_off, 2 * 65536); qp[3] = ld_sc(rs_ws, qp_off, 3 * 65536); UPTO(7); KSTORE(7)
  WAITG(8); SB(); qp[4] = ld_sc(rs_ws, qp_off, 4 * 65536); qp[5] = ld_sc(rs_ws, qp_off, 5 * 65536); UPTO(8); KSTORE(8)
  WAITG(9); SB(); qp[6] = ld_sc(rs_ws, qp_off, 6 * 65536); qp[7] = ld_sc(rs_ws, qp_off, 7 * 65536); qc = ld_sc(rs_ws, qc_off, 0); UPTO(9); KSTORE(9)
  WAITG(10); UPTO(10);
#define QBAD(bad)                                                                                   \
  {                                                                                                 \
    u32 x_ = (tid & 31) < 8 ? qc[2] : 0u;                                                           \
    _Pragma("unroll") for (int s = 0; s < 8; ++s) x_ ^= qp[s][0] ^ qp[s][1] ^ qp[s][2] ^ qp[s][3];  \
    bad = half_xor(x_) | (qc[0] ^ (u32)epoch) | (qc[1] ^ (u32)(epoch >> 32));                       \
  }
  {
    u32 bad;
    QBAD(bad)
    if (!__all(bad == 0)) {
      for (int spins = 0; spins < SPIN_CAP; ++spins) {
        asm volatile("" ::: "memory");
#pragma unroll
        for (int s = 0; s < 8; ++s) qp[s] = ld_sc(rs_ws, qp_off, s * 65536);
        qc = ld_sc(rs_ws, qc_off, 0);
        QBAD(bad)
        if (__all(bad == 0)) break;
      }
    }
    f32x4 a = __builtin_bit_cast(f32x4, qp[0]);
#pragma unroll
    for (int s = 1; s < 8; ++s) a += __builtin_bit_cast(f32x4, qp[s]);
    *(u32x2*)(lds + L_QS + (tid >> 5) * 272 + (tid & 31) * 8) = u32x2{cvt_pk(a[0], a[1]), cvt_pk(a[2], a[3])};
  }
  wg_barrier();

  u32x4 pf[2];
  float m_w, l_w;
  {
    u32x4 qlo[4], qhi[4];
#pragma unroll
    for (int s = 0; s < 4; ++s) {
      const u32x4 qv = *(const u32x4*)(lds + L_QS + (c & 7) * 272 + (4 * s + g) * 16);
      const u32x4 z = {0u, 0u, 0u, 0u};
      qlo[s] = c < 8 ? qv : z; qhi[s] = c < 8 ? z : qv;
    }
    const int rho = (c & 3) * 4 + (c >> 2);
    float sc[4][4];
#pragma unroll
    for (int u = 0; u < 2; ++u) {
#pragma unroll
      for (int a = 0; a < 2; ++a) {
        f32x4 acc = {0.f, 0.f, 0.f, 0.f};
#pragma unroll
        for (int s = 0; s < 4; ++s) {
          const u32x4 kf = *(const u32x4*)(kl + ((u * 4 + a) * 16 + rho) * 256 + (((4 * s + g) ^ c) * 16));
          acc = mfma(kf, qlo[s], acc);
        }
#pragma unroll
        for (int s = 0; s < 4; ++s) {
          const u32x4 kf = *(const u32x4*)(kl + ((u * 4 + 2 + a) * 16 + rho) * 256 + (((4 * s + g) ^ c) * 16));
          acc = mfma(kf, qhi[s], acc);
        }
        const u32 p01 = cvt_pk(acc[0], acc[1]), p23 = cvt_pk(acc[2], acc[3]);
        const u32 r01 = cvt_pk(bf_lo(p01) * 0.08838834764831845f, bf_hi(p01) * 0.08838834764831845f);
        const u32 r23 = cvt_pk(bf_lo(p23) * 0.08838834764831845f, bf_hi(p23) * 0.08838834764831845f);
        sc[u * 2 + a][0] = bf_lo(r01); sc[u * 2 + a][1] = bf_hi(r01); sc[u * 2 + a][2] = bf_lo(r23); sc[u * 2 + a][3] = bf_hi(r23);
      }
      if (u == 0) { WAITG(11); UPTO(11); }
    }
    float m = sc[0][0];
#pragma unroll
    for (int a = 0; a < 4; ++a)
#pragma unroll
      for (int i = 0; i < 4; ++i) m = fmaxf(m, sc[a][i]);
    m = fmaxf(m, dpp<XOR8>(m));
    m = xrow_max(m);
    const float ml2 = m * 1.4426950408889634f;
    float l = 0.f;
#pragma unroll
    for (int a = 0; a < 4; ++a) {
#pragma unroll
      for (int i = 0; i < 4; ++i) { sc[a][i] = __builtin_amdgcn_exp2f(fmaf(sc[a][i], 1.4426950408889634f, -ml2)); l += sc[a][i]; }
    }
#pragma unroll
    for (int u = 0; u < 2; ++u) {
      pf[u][0] = cvt_pk(sc[2 * u][0], sc[2 * u][1]);
      pf[u][1] = cvt_pk(sc[2 * u][2], sc[2 * u][3]);
      pf[u][2] = cvt_pk(sc[2 * u + 1][0], sc[2 * u + 1][1]);
      pf[u][3] = cvt_pk(sc[2 * u + 1][2], sc[2 * u + 1][3]);
    }
    l += dpp<XOR8>(l);
    m_w = m; l_w = xrow_sum(l);
  }

  f32x4 oa[8];
  u32x4 pl[2], ph[2];
  {
    const u32x4 z = {0u, 0u, 0u, 0u};
#pragma unroll
    for (int u = 0; u < 2; ++u) { pl[u] = c < 8 ? pf[u] : z; ph[u] = c < 8 ? z : pf[u]; }
  }
  WAITG(12); UPTO(12);
#pragma unroll
  for (int i = 0; i < 8; ++i) oa[i] = mfma(colfrag(vb[0], i), pl[0], f32x4{0.f, 0.f, 0.f, 0.f});
  WAITG(13); UPTO(13);
#pragma unroll
  for (int i = 0; i < 8; ++i) oa[i] = mfma(colfrag(vb[1], i), ph[0], oa[i]);
  WAITG(14); SB();
  u32 clm = at_add(1u, rs_ws, tid == 0 ? WS_CF + shard * 256 : OOB);
  UPTO(14);
  WAITG(15); UPTO(15);
#pragma unroll
  for (int i = 0; i < 8; ++i) oa[i] = mfma(colfrag(vb[2], i), pl[1], oa[i]);
  WAITG(16);
  if (tid == 0 && (clm & 0xfffff000u) != QTAG) {
    for (int spins = 0; spins < SPIN_CAP; ++spins) {
      asm volatile("" ::: "memory");
      if ((__builtin_amdgcn_raw_buffer_load_b32(rs_ws, WS_CF + shard * 256, 0, 16) & 0xfffff000u) == QTAG) break;
    }
    clm = at_add(1u, rs_ws, WS_CF + shard * 256);
  }
  if (tid == 0) *(u32*)(lds + L_CL) = min(clm & 0xfffu, (u32)(256 / NSH - 1));
  wg_barrier();
  const int kq = __builtin_amdgcn_readfirstlane(*(const u32*)(lds + L_CL));
  constexpr int PF = 224 / NSH, PS = 32 / NSH;
  const bool has_f = kq < PF, has_d = kq < PF - PS || kq >= PF;
  const int tfc = has_f ? shard * PF + kq : 0;
  const int kgf = tfc / 28, cgf = tfc - kgf * 28;
  rs_wf = mk(((kgf & 1) ? p.wu : p.wg) + (size_t)(256 * (kgf >> 1)) * 7168 + cgf * 256, has_f ? 256 * 7168 - cgf * 256 : 0);
  const int tdn = shard * PF + (kq >= PF ? kq - PF : (has_d ? kq + PS : 0));
  const int rgd = tdn >> 3, cgd = tdn & 7;
  rs_wd = mk(p.wd + (size_t)rgd * 128 * 2048 + cgd * 256, has_d ? 128 * 2048 - cgd * 256 : 0);
  const rsrc_t rs_wsd = mk(p.ws, has_d ? WS_END : 0u);
  UPTO(16);
  WAITG(17); UPTO(17);
#pragma unroll
  for (int i = 0; i < 8; ++i) oa[i] = mfma(colfrag(vb[3], i), ph[1], oa[i]);
  {
    char* d = kl + LK_MRG + c * 512 + g * 128;
#pragma unroll
    for (int i = 0; i < 8; ++i) *(f32x4*)(d + (i ^ (c & 7)) * 16) = oa[i];
    if (lane < 8) *(float2*)(lds + L_ML + (wave * 8 + c) * 8) = make_float2(m_w, l_w);
  }
  wg_barrier();
  {
    const int hh = tid >> 5, d4 = tid & 31;
    float2 ml[4];
#pragma unroll
    for (int w = 0; w < 4; ++w) ml[w] = *(const float2*)(lds + L_ML + (w * 8 + hh) * 8);
    const float M = fmaxf(fmaxf(ml[0].x, ml[1].x), fmaxf(ml[2].x, ml[3].x));
    f32x4 o = {0.f, 0.f, 0.f, 0.f};
    float L = 0.f;
#pragma unroll
    for (int w = 0; w < 4; ++w) {
      const float f = fexp(ml[w].x - M);
      L = fmaf(f, ml[w].y, L);
      const char* s = lds + L_K + w * 32768 + LK_MRG + hh * 512 + (d4 ^ hh) * 16;
      o += f * (*(const f32x4*)s + *(const f32x4*)(s + 4096));
    }
    o *= frcp(L);
    const u32 p0 = cvt_pk(o[0], o[1]), p1 = cvt_pk(o[2], o[3]);
    const u32x4 pk = {p0, p1, dppu<XOR1>(p0), dppu<XOR1>(p1)};
    const u32 xr = half_xor(p0 ^ p1) ^ __float_as_uint(M) ^ __float_as_uint(L) ^ (u32)(epoch >> 32);
    const u32 slot = (u32)((b_att * 32 + kvh * 8 + hh) * 16 + split);
    st_sc(pk, rs_ws, (d4 & 1) == 0 ? WS_OP + slot * 256 + d4 * 8 : OOB, 0);
    st_sc(u32x4{__float_as_uint(M), __float_as_uint(L), xr, (u32)epoch}, rs_ws, d4 == 0 ? WS_ML + slot * 16 : OOB, 0);
  }

  u32x4 og[4], mlv;
  const u32 og_off = WS_OP + (u32)((wave * 32 + rgo) * 16 + g * 4) * 256 + c * 16;
  const u32 ml_off = WS_ML + (u32)((wave * 32 + rgo) * 16 + ((c + g * 4) & 15)) * 16;
  {
    f32x4 acc[2] = {{0.f, 0.f, 0.f, 0.f}, {0.f, 0.f, 0.f, 0.f}};
    const u32x4 xfa = *(const u32x4*)(lds + L_XN + (c & 3) * 2064 + (kgf >> 1) * 512 + wave * 64 + g * 16);
    const u32x4 xfb = *(const u32x4*)(lds + L_XN + (c & 3) * 2064 + (kgf >> 1) * 512 + 256 + wave * 64 + g * 16);
    u32x4 ama[4], amb[4];
    amask(xfa, c, ama); amask(xfb, c, amb);
    WAITG(18); UPTO(18);
    WAITG(19); UPTO(19);
#pragma unroll
    for (int i = 0; i < 8; ++i) acc[i >> 2] = mfma(ama[i & 3], colfrag(fa, i), acc[i >> 2]);
    WAITG(20); UPTO(20);
    WAITG(21); SB();
#pragma unroll
    for (int i = 0; i < 8; ++i) acc[i >> 2] = mfma(amb[i & 3], colfrag(fb, i), acc[i >> 2]);
    tile_store2(kl + LK_RF, acc, c, g);
  }
  wg_barrier();
  {
    const char* s = lds + L_K + LK_RF + tile_off(tid & 127);
    f32x4 a = (*(const f32x4*)s + *(const f32x4*)(s + 32768)) + (*(const f32x4*)(s + 65536) + *(const f32x4*)(s + 98304));
    st_sc(tag4(a, T12), rs_ws, (has_f && tid < 128) ? WS_GU + (u32)(kgf * DF + cgf * 128 + tid) * 16 : OOB, 0);
  }
  SB();
#pragma unroll
  for (int s = 0; s < 4; ++s) og[s] = ld_sc(rs_ws, og_off, s * 256);
  mlv = ld_sc(rs_ws, ml_off, 0);
  UPTO(21);
  SB();
  WAITG(22); UPTO(22);
#define OBAD(bad)                                                                                                  \
  {                                                                                                                \
    const u32 want = mlv[2] ^ mlv[0] ^ mlv[1] ^ (u32)(epoch >> 32);              \
    u32 x_ = c < 4 ? want : 0u;                                         \
    _Pragma("unroll") for (int s = 0; s < 4; ++s) x_ ^= og[s][0] ^ og[s][1] ^ og[s][2] ^ og[s][3];                 \
    bad = row_xor(x_) | (mlv[3] ^ (u32)epoch);                                                                     \
  }
  {
    u32 bad;
    OBAD(bad)
    if (!__all(bad == 0)) {
      for (int spins = 0; spins < SPIN_CAP; ++spins) {
        asm volatile("" ::: "memory");
#pragma unroll
        for (int s = 0; s < 4; ++s) og[s] = ld_sc(rs_ws, og_off, s * 256);
        mlv = ld_sc(rs_ws, ml_off, 0);
        OBAD(bad)
        if (__all(bad == 0)) break;
      }
    }
  }
  {
    const float ms = __uint_as_float(mlv[0]), ls = __uint_as_float(mlv[1]);
    const float M = row16_max(ms);
    const float wl = fexp(ms - M) * ls;
    const float w = wl * frcp(row16_sum(wl));
    const float w0 = dpp<0x150>(w), w1 = dpp<0x151>(w), w2 = dpp<0x152>(w), w3 = dpp<0x153>(w);
    u32x4 pk;
#pragma unroll
    for (int i = 0; i < 4; ++i) {
      float lo = w0 * bf_lo(og[0][i]), hi = w0 * bf_hi(og[0][i]);
      lo = fmaf(w1, bf_lo(og[1][i]), lo); hi = fmaf(w1, bf_hi(og[1][i]), hi);
      lo = fmaf(w2, bf_lo(og[2][i]), lo); hi = fmaf(w2, bf_hi(og[2][i]), hi);
      lo = fmaf(w3, bf_lo(og[3][i]), lo); hi = fmaf(w3, bf_hi(og[3][i]), hi);
      pk[i] = cvt_pk(xrow_sum(lo), xrow_sum(hi));
    }
    if (lane < 16) *(u32x4*)(lds + L_OS + wave * 256 + lane * 16) = pk;
  }
  WAITG(23);
  u32x4 gp[4];
  const u32 gu_off = WS_GU + (u32)((lane >> 5) * DF + rgd * 128 + wave * 32 + (lane & 31)) * 16;
  asm volatile("" ::: "memory");
#pragma unroll
  for (int s = 0; s < 4; ++s) gp[s] = ld_sc(rs_wsd, gu_off, s * (2 * DF * 16));
  SB();
  UPTO(23);
  u32x4 of_[8];
#pragma unroll
  for (int i = 0; i < 8; ++i) of_[i] = colfrag(wo, i);
  wg_barrier();
  {
    const u32x4 of = *(const u32x4*)(lds + L_OS + (c & 3) * 256 + wave * 64 + g * 16);
    f32x4 acc[2] = {{0.f, 0.f, 0.f, 0.f}, {0.f, 0.f, 0.f, 0.f}};
    u32x4 am[4];
    amask(of, c, am);
#pragma unroll
    for (int i = 0; i < 8; ++i) acc[i & 1] = mfma(am[i >> 1], of_[(i & 1) * 4 + (i >> 1)], acc[i & 1]);
    tile_store2(kl + LK_RO, acc, c, g);
  }
  wg_barrier();
  {
    const char* s = lds + L_K + LK_RO + tile_off(tid & 127);
    f32x4 a = (*(const f32x4*)s + *(const f32x4*)(s + 32768)) + (*(const f32x4*)(s + 65536) + *(const f32x4*)(s + 98304));
    st_sc(tag4(a, T12), rs_ws, tid < 128 ? WS_FP + (u32)(rgo * 1024 + cgo * 128 + tid) * 16 : OOB, 0);
  }
  SB();
  {
    u32 bad = 0;
#pragma unroll
    for (int s = 0; s < 4; ++s) bad |= tagbad(gp[s], T12);
    if (has_d && !__all(bad == 0)) {
      for (int spins = 0; spins < SPIN_CAP; ++spins) {
        asm volatile("" ::: "memory");
#pragma unroll
        for (int s = 0; s < 4; ++s) gp[s] = ld_sc(rs_wsd, gu_off, s * (2 * DF * 16));
        bad = 0;
#pragma unroll
        for (int s = 0; s < 4; ++s) bad |= tagbad(gp[s], T12);
        if (__all(bad == 0)) break;
      }
    }
    const f32x4 a = (untag(gp[0]) + untag(gp[1])) + (untag(gp[2]) + untag(gp[3]));
#pragma unroll
    for (int b = 0; b < 4; ++b) {
      const float v = rbf(a[b]);
      auto t = __builtin_amdgcn_permlane32_swap(__float_as_uint(v), __float_as_uint(v), false, false);
      const float gv = __uint_as_float(t[0]), uv = __uint_as_float(t[1]);
      const float inner = 0.7978845608028654f * (gv + 0.044715f * gv * gv * gv);
      const float th = 1.f - 2.f * frcp(fexp(2.f * inner) + 1.f);
      const float ge = rbf(0.5f * gv * (1.f + th));
      const u32 hv = cvt_pk(ge * uv, 0.f);
      if (lane < 32) *(unsigned short*)(kl + LK_HS + b * 64 + lane * 2) = (unsigned short)hv;
    }
  }
  asm volatile("s_waitcnt lgkmcnt(0)" ::: "memory");
  SB();
  u32x4 df[8];
#pragma unroll
  for (int i = 0; i < 8; ++i) df[i] = colfrag(wd, i);
  {
    const u32x4 hf = *(const u32x4*)(kl + LK_HS + (c & 3) * 64 + g * 16);
    f32x4 acc[2] = {{0.f, 0.f, 0.f, 0.f}, {0.f, 0.f, 0.f, 0.f}};
    u32x4 am[4];
    amask(hf, c, am);
#pragma unroll
    for (int i = 0; i < 8; ++i) acc[i & 1] = mfma(am[i >> 1], df[(i & 1) * 4 + (i >> 1)], acc[i & 1]);
    tile_store2(kl + LK_RD, acc, c, g);
  }
  wg_barrier();
  {
    const char* s = lds + L_K + LK_RD + tile_off(tid & 127);
    f32x4 a = (*(const f32x4*)s + *(const f32x4*)(s + 32768)) + (*(const f32x4*)(s + 65536) + *(const f32x4*)(s + 98304));
    st_sc(tag4(a, T12), rs_wsd, tid < 128 ? WS_FP + (u32)((32 + rgd) * 1024 + cgd * 128 + tid) * 16 : OOB, 0);
  }
  SB();
  {
    const int col0 = (xcd * 32 + r) * 4;
    const int ks = tid >> 2, cc = tid & 3;
    const bool act = ks < 60;
    const u32 off = act ? WS_FP + (u32)(ks * 1024 + col0 + cc) * 16 : OOB;
    const unsigned short xin = *(const unsigned short*)(p.x + (((tid >> 2) & 3) * DM + col0 + cc) * 2);
    u32x4 fv;
    for (int spins = 0; spins < SPIN_CAP; ++spins) {
      fv = ld_sc(rs_ws, off, 0);
      const u32 bad = act ? tagbad(fv, T12) : 0u;
      if (__all(bad == 0)) break;
      asm volatile("" ::: "memory");
    }
    f32x4 a = untag(fv);
#pragma unroll
    for (int i = 0; i < 4; ++i) {
      float t = a[i];
      t += dpp<0x124>(t);
      t += dpp<XOR8>(t);
      a[i] = xrow_sum(t);
    }
    if (lane < 4) *(f32x4*)(lds + L_K + LK_FIN + (wave * 4 + lane) * 16) = a;
    wg_barrier();
    if (tid < 16) {
      const int b = tid >> 2, cc2 = tid & 3;
      const float* fs = (const float*)(lds + L_K + LK_FIN) + cc2 * 4 + b;
      const float s = rbf(fs[0] + fs[16]);
      const float dd = rbf(fs[32] + fs[48]);
      const float gt = rbf(dd * frcp(1.f + fexp(-dd)));
      const float o = bf_lo(xin) + rbf(s * gt);
      *(unsigned short*)(p.out + (b * DM + col0 + cc2) * 2) = (unsigned short)cvt_pk(o, o);
    }
    if (bid == 0 && tid == 0) st_sc64(u32x2{(u32)epoch, (u32)(epoch >> 32)}, rs_ws, WS_E, 0);
  }
  KEEP(cw0);
  asm volatile(".Lfdk_end:");
}

extern "C" void kernel_launch(void* const* d_in, const int* in_sizes, int n_in, void* d_out, int out_size, void* d_ws,
                              size_t ws_size, hipStream_t stream) {
  Params p;
  p.x = (const char*)d_in[0]; p.k = (const char*)d_in[1]; p.v = (const char*)d_in[2];
  p.wq = (const char*)d_in[3]; p.wo = (const char*)d_in[4];
  p.wg = (const char*)d_in[6]; p.wu = (const char*)d_in[7]; p.wd = (const char*)d_in[8];
  p.out = (char*)d_out; p.ws = (char*)d_ws;
  (void)hipFuncSetAttribute((const void*)fused_decode_kernel, hipFuncAttributeMaxDynamicSharedMemorySize, L_END);
  fused_decode_kernel<<<NWG, NTHR, L_END, stream>>>(p);
}
```

```cpp
#include <hip/hip_runtime.h>
#include <stdint.h>

typedef unsigned u32;
typedef unsigned long long u64;
typedef u32 u32x4 __attribute__((ext_vector_type(4)));
typedef u32 u32x2 __attribute__((ext_vector_type(2)));
typedef float f32x4 __attribute__((ext_vector_type(4)));
typedef __bf16 bf16x8 __attribute__((ext_vector_type(8)));
typedef __amdgpu_buffer_rsrc_t rsrc_t;

constexpr int NB = 4, DM = 1024, SK = 8192, DF = 3584;
constexpr int NWG = 256, NTHR = 256;

constexpr u32 WS_E = 0;
constexpr u32 WS_CF = 1024;
constexpr u32 WS_QC = 4096;
constexpr u32 WS_QP = 32768;
constexpr u32 WS_GU = WS_QP + 8 * 4 * 4096 * 4;
constexpr u32 WS_OP = WS_GU + 4 * 2 * DF * 16;
constexpr u32 WS_ML = WS_OP + 4 * 32 * 16 * 256;
constexpr u32 WS_FP = WS_ML + 4 * 32 * 16 * 16;
constexpr u32 WS_END = WS_FP + 60 * 1024 * 16;

constexpr int L_K = 0;
constexpr int L_REDQ = 131072;
constexpr int L_XN = L_REDQ + 8192;
constexpr int L_QS = L_XN + 4 * 2064;
constexpr int L_OS = L_QS + 8 * 272;
constexpr int L_ML = L_OS + 1024;
constexpr int L_CL = L_ML + 256;
constexpr int L_END = L_CL + 16;
constexpr int LK_MRG = 0;
constexpr int LK_RF = 8192;
constexpr int LK_RD = 10240;
constexpr int LK_RO = 12288;
constexpr int LK_HS = 14336;
constexpr int LK_FIN = 16384;

struct Params {
  const char* x; const char* k; const char* v; const char* wq; const char* wo;
  const char* wg; const char* wu; const char* wd; char* out; char* ws;
};

#define SB() __builtin_amdgcn_sched_barrier(0)
#define KEEP(x) do { SB(); asm volatile("" ::"v"(x)); } while (0)

__device__ __forceinline__ rsrc_t mk(const char* p, u32 bytes) {
  return __builtin_amdgcn_make_buffer_rsrc((void*)p, 0, bytes, 0x00020000);
}
__device__ __forceinline__ u32x4 ld_nt(rsrc_t r, u32 voff, u32 soff) { return __builtin_amdgcn_raw_buffer_load_b128(r, voff, soff, 2); }
__device__ __forceinline__ u32x4 ld_pl(rsrc_t r, u32 voff, u32 soff) { return __builtin_amdgcn_raw_buffer_load_b128(r, voff, soff, 0); }
__device__ __forceinline__ u32x4 ld_sc(rsrc_t r, u32 voff, u32 soff) { return __builtin_amdgcn_raw_buffer_load_b128(r, voff, soff, 16); }
__device__ __forceinline__ u32x2 ld_sc64(rsrc_t r, u32 voff, u32 soff) { return __builtin_amdgcn_raw_buffer_load_b64(r, voff, soff, 16); }
__device__ __forceinline__ void st_sc(u32x4 v, rsrc_t r, u32 voff, u32 soff) { __builtin_amdgcn_raw_buffer_store_b128(v, r, voff, soff, 16); }
__device__ __forceinline__ void st_sc64(u32x2 v, rsrc_t r, u32 voff, u32 soff) { __builtin_amdgcn_raw_buffer_store_b64(v, r, voff, soff, 16); }

__device__ __forceinline__ void wg_barrier() {
  asm volatile("s_waitcnt lgkmcnt(0)\n\ts_barrier" ::: "memory");
}
__device__ __forceinline__ float bf_lo(u32 v) { return __uint_as_float(v << 16); }
__device__ __forceinline__ float bf_hi(u32 v) { return __uint_as_float(v & 0xffff0000u); }
typedef __bf16 bf16x2 __attribute__((ext_vector_type(2)));
typedef float f32x2 __attribute__((ext_vector_type(2)));
__device__ __forceinline__ u32 cvt_pk(float lo, float hi) {
  return __builtin_bit_cast(u32, __builtin_convertvector(f32x2{lo, hi}, bf16x2));
}
__device__ __forceinline__ float rbf(float f) { return bf_lo(cvt_pk(f, f)); }

template <int CTRL> __device__ __forceinline__ float dpp(float x) {
  return __builtin_bit_cast(float, __builtin_amdgcn_mov_dpp(__builtin_bit_cast(int, x), CTRL, 0xf, 0xf, true));
}
constexpr int XOR1 = 0xB1, XOR2 = 0x4E, XOR7 = 0x141, XOR8 = 0x128;
__device__ __forceinline__ float xrow_sum(float x) {
  auto s = __builtin_amdgcn_permlane16_swap(__float_as_uint(x), __float_as_uint(x), false, false);
  x = __uint_as_float(s[0]) + __uint_as_float(s[1]);
  auto t = __builtin_amdgcn_permlane32_swap(__float_as_uint(x), __float_as_uint(x), false, false);
  return __uint_as_float(t[0]) + __uint_as_float(t[1]);
}
__device__ __forceinline__ float xrow_max(float x) {
  auto s = __builtin_amdgcn_permlane16_swap(__float_as_uint(x), __float_as_uint(x), false, false);
  x = fmaxf(__uint_as_float(s[0]), __uint_as_float(s[1]));
  auto t = __builtin_amdgcn_permlane32_swap(__float_as_uint(x), __float_as_uint(x), false, false);
  return fmaxf(__uint_as_float(t[0]), __uint_as_float(t[1]));
}
__device__ __forceinline__ float wave_sum(float x) {
  x += dpp<XOR1>(x); x += dpp<XOR2>(x); x += dpp<XOR7>(x); x += dpp<XOR8>(x);
  return xrow_sum(x);
}
__device__ __forceinline__ float row16_max(float x) {
  x = fmaxf(x, dpp<XOR1>(x)); x = fmaxf(x, dpp<XOR2>(x)); x = fmaxf(x, dpp<XOR7>(x)); x = fmaxf(x, dpp<XOR8>(x));
  return x;
}
__device__ __forceinline__ float row16_sum(float x) {
  x += dpp<XOR1>(x); x += dpp<XOR2>(x); x += dpp<XOR7>(x); x += dpp<XOR8>(x);
  return x;
}
__device__ __forceinline__ float fexp(float x) { return __builtin_amdgcn_exp2f(x * 1.4426950408889634f); }

__device__ __forceinline__ f32x4 mfma(u32x4 a, u32x4 b, f32x4 c) {
  return __builtin_amdgcn_mfma_f32_16x16x32_bf16(__builtin_bit_cast(bf16x8, a), __builtin_bit_cast(bf16x8, b), c, 0, 0, 0);
}
__device__ __forceinline__ u32x4 colfrag(const u32x4 (&w)[8], int i) {
  const u32 sel = (i & 1) ? 0x07060302u : 0x05040100u;
  const int d = i >> 1;
  u32x4 f;
  f[0] = __builtin_amdgcn_perm(w[1][d], w[0][d], sel);
  f[1] = __builtin_amdgcn_perm(w[3][d], w[2][d], sel);
  f[2] = __builtin_amdgcn_perm(w[5][d], w[4][d], sel);
  f[3] = __builtin_amdgcn_perm(w[7][d], w[6][d], sel);
  return f;
}
__device__ __forceinline__ u32 tag1(float v, u32 t) { return ((__float_as_uint(v) + 0x800u) & 0xfffff000u) | t; }
__device__ __forceinline__ u32x4 tag4(f32x4 v, u32 t) { u32x4 r = {tag1(v[0], t), tag1(v[1], t), tag1(v[2], t), tag1(v[3], t)}; return r; }
__device__ __forceinline__ u32 tagbad(u32x4 v, u32 t) { return ((v[0] ^ t) | (v[1] ^ t) | (v[2] ^ t) | (v[3] ^ t)) & 0xfffu; }
__device__ __forceinline__ f32x4 untag(u32x4 v) {
  f32x4 r = {__uint_as_float(v[0] & 0xfffff000u), __uint_as_float(v[1] & 0xfffff000u), __uint_as_float(v[2] & 0xfffff000u), __uint_as_float(v[3] & 0xfffff000u)};
  return r;
}
constexpr int SPIN_CAP = 1 << 18;
constexpr int NSH = 4;
__device__ __forceinline__ u32 at_add(u32 v, rsrc_t r, u32 voff) {
  return (u32)__builtin_amdgcn_raw_ptr_buffer_atomic_add_i32((int)v, r, voff, 0, 0);
}
constexpr u32 OOB = 0x80000000u;
template <int CTRL> __device__ __forceinline__ u32 dppu(u32 x) { return (u32)__builtin_amdgcn_mov_dpp((int)x, CTRL, 0xf, 0xf, true); }
__device__ __forceinline__ u32 half_xor(u32 x) {
  x ^= dppu<XOR1>(x); x ^= dppu<XOR2>(x); x ^= dppu<XOR7>(x); x ^= dppu<XOR8>(x);
  auto t = __builtin_amdgcn_permlane16_swap(x, x, false, false);
  return t[0] ^ t[1];
}
__device__ __forceinline__ u32 row_xor(u32 x) {
  x ^= dppu<XOR1>(x); x ^= dppu<XOR2>(x); x ^= dppu<XOR7>(x); x ^= dppu<XOR8>(x);
  return x;
}
__device__ __forceinline__ float frcp(float x) { return __builtin_amdgcn_rcpf(x); }
__device__ __forceinline__ void amask(u32x4 a, int c, u32x4 (&am)[4]) {
  const u32x4 z = {0u, 0u, 0u, 0u};
#pragma unroll
  for (int q = 0; q < 4; ++q) am[q] = (c >> 2) == q ? a : z;
}
__device__ __forceinline__ void tile_store2(char* d, const f32x4 (&acc)[2], int c, int g) {
#pragma unroll
  for (int j = 0; j < 2; ++j) *(f32x4*)(d + (c * 8 + ((j * 4 + g) ^ (c & 7))) * 16) = acc[j];
}
__device__ __forceinline__ int tile_off(int col) { return (col ^ ((col >> 3) & 7)) * 16; }

__global__ void __launch_bounds__(NTHR, 1) fused_decode_kernel(Params p) {
  extern __shared__ __attribute__((aligned(16))) char lds[];
  const int tid = threadIdx.x, lane = tid & 63;
  const int wave = __builtin_amdgcn_readfirstlane(tid >> 6);
  const int c = lane & 15, g = lane >> 4;
  const int bid = blockIdx.x, xcd = bid & 7, r = bid >> 3;

  u32 cw0;
  {
    u64 pc; u32 len;
    asm volatile("s_getpc_b64 %0\n.Lfdk_here:\n\ts_mov_b32 %1, .Lfdk_end-.Lfdk_here" : "=s"(pc), "=s"(len));
    len = (int)len > 0 ? len : 0u;
    const rsrc_t rs_c = mk((const char*)pc, len);
    cw0 = __builtin_amdgcn_raw_buffer_load_b32(rs_c, tid * 128, 0, 0);
  }
  SB();
  const int pair = xcd * 2 + (r >> 4), b_att = pair >> 2, kvh = pair & 3, split = r & 15;
  const rsrc_t rs_ws = mk(p.ws, WS_END);
  const rsrc_t rs_x = mk(p.x, NB * DM * 2);
  const rsrc_t rs_wq = mk(p.wq + (size_t)(128 * xcd) * 8192 + r * 256, 128 * 8192 - r * 256);
  const rsrc_t rs_k = mk(p.k + ((size_t)pair * SK + split * 512) * 256, 512 * 256);

  u32 tw0, tw1, tw2, tw3, tw4;
  {
    const char* a0 = p.wq + (size_t)(128 * xcd) * 8192 + r * 256;
    const char* a1 = p.k + ((size_t)pair * SK + split * 512) * 256;
    const char* a2 = p.x;
    const char* a3 = p.wq + (size_t)(128 * xcd + 127) * 8192 + r * 256;
    const char* a4 = p.ws;
    asm volatile("s_load_dword %0, %5, 0x0\n\ts_load_dword %1, %6, 0x0\n\ts_load_dword %2, %7, 0x0\n\ts_load_dword %3, %8, 0x0\n\ts_load_dword %4, %9, 0x0"
                 : "=&s"(tw0), "=&s"(tw1), "=&s"(tw2), "=&s"(tw3), "=&s"(tw4) : "s"(a0), "s"(a1), "s"(a2), "s"(a3), "s"(a4));
  }
  u32x2 e2 = ld_sc64(rs_ws, WS_E, 0);
  u32x4 x0 = ld_pl(rs_x, wave * 2048 + lane * 32, 0);
  u32x4 x1 = ld_pl(rs_x, wave * 2048 + lane * 32 + 16, 0);
  SB();
  u32x4 wq[8], kb[32], vb[4][8], fa[8], fb[8], wo[8], wd[8];
  const u32 vo8 = (u32)(wave * 32 + g * 8) * 8192 + c * 16;
  const u32 vok = (u32)wave * 1024 + lane * 16;
#define ISSUE(n)                                                                                              \
  do {                                                                                                        \
    SB();                                                                                                     \
    _Pragma("unroll") for (int j_ = 0; j_ < 4; ++j_) {                                                        \
      if ((n) < 2) wq[(n) * 4 + j_] = ld_nt(rs_wq, vo8, ((n) * 4 + j_) * 8192);                               \
      else if ((n) < 10) kb[((n) - 2) * 4 + j_] = ld_nt(rs_k, vok, (((n) - 2) * 4 + j_) * 4096);              \
      else if ((n) < 18) vb[((n) - 10) / 2][(((n) - 10) & 1) * 4 + j_] = ld_nt(rs_v, vok, (((n) - 10) * 4 + j_) * 4096); \
      else if ((n) < 20) fa[((n) - 18) * 4 + j_] = ld_nt(rs_wf, vo7, (((n) - 18) * 4 + j_) * 7168);           \
      else if ((n) < 22) fb[((n) - 20) * 4 + j_] = ld_nt(rs_wf, vo7, (128 + ((n) - 20) * 4 + j_) * 7168);     \
      else if ((n) < 24) wo[((n) - 22) * 4 + j_] = ld_nt(rs_wo, vo2o, (j_ * 8 + ((n) - 22)) * 2048);          \
      else wd[((n) - 24) * 4 + j_] = ld_nt(rs_wd, vo2, (((n) - 24) * 4 + j_) * 2048);                         \
    }                                                                                                         \
    SB();                                                                                                     \
  } while (0)
#define ISSUE_A(n)                                                                                            \
  do {                                                                                                        \
    SB();                                                                                                     \
    _Pragma("unroll") for (int j_ = 0; j_ < 4; ++j_) {                                                        \
      if ((n) < 2) wq[(n) * 4 + j_] = ld_nt(rs_wq, vo8, ((n) * 4 + j_) * 8192);                               \
      else kb[((n) - 2) * 4 + j_] = ld_nt(rs_k, vok, (((n) - 2) * 4 + j_) * 4096);                            \
    }                                                                                                         \
    SB();                                                                                                     \
  } while (0)
#define WAITG(n)                                                                                              \
  do {                                                                                                        \
    if ((n) < 2) KEEP(wq[(n) * 4 + 3]);                                                                       \
    else if ((n) < 10) KEEP(kb[((n) - 2) * 4 + 3]);                                                           \
    else if ((n) < 18) KEEP(vb[((n) - 10) / 2][(((n) - 10) & 1) * 4 + 3]);                                    \
    else if ((n) < 20) KEEP(fa[((n) - 18) * 4 + 3]);                                                          \
    else if ((n) < 22) KEEP(fb[((n) - 20) * 4 + 3]);                                                          \
    else if ((n) < 24) KEEP(wo[((n) - 22) * 4 + 3]);                                                          \
    else KEEP(wd[((n) - 24) * 4 + 3]);                                                                        \
  } while (0)
  constexpr int NXT[25] = {2, 3, 4, 4, 5, 6, 7, 8, 9, 10, 11, 13, 14, 15, 15, 16, 17, 18, 19, 20, 21, 22, 23, 24, 25};
#define UPTO(n)                                              \
  do {                                                       \
    if (NXT[n] + 1 <= NXT[(n) + 1]) ISSUE(NXT[n] + 1);       \
    if (NXT[n] + 2 <= NXT[(n) + 1]) ISSUE(NXT[n] + 2);       \
    if (NXT[n] + 3 <= NXT[(n) + 1]) ISSUE(NXT[n] + 3);       \
  } while (0)
  ISSUE_A(0); ISSUE_A(1); ISSUE_A(2);

  const int rgo = xcd * 4 + (r >> 3), cgo = r & 7;
  const rsrc_t rs_v = mk(p.v + ((size_t)pair * SK + split * 512) * 256, 512 * 256);
  const rsrc_t rs_wo = mk(p.wo + (size_t)rgo * 128 * 2048 + cgo * 256, 128 * 2048 - cgo * 256);
  rsrc_t rs_wf = rs_x, rs_wd = rs_x;
  const u32 vo7 = (u32)(wave * 32 + g * 8) * 7168 + c * 16;
  const u32 vo2 = (u32)(wave * 32 + g * 8) * 2048 + c * 16;
  const u32 vo2o = (u32)(wave * 32 + g * 2) * 2048 + c * 16;
  const int shard = ((r >> 2) + r + xcd) & (NSH - 1);

  {
    float v[16];
#pragma unroll
    for (int i = 0; i < 4; ++i) { v[2 * i] = bf_lo(x0[i]); v[2 * i + 1] = bf_hi(x0[i]); v[8 + 2 * i] = bf_lo(x1[i]); v[8 + 2 * i + 1] = bf_hi(x1[i]); }
    float s = 0.f;
#pragma unroll
    for (int i = 0; i < 16; ++i) s += v[i];
    const float mean = wave_sum(s) * (1.f / DM);
    float q = 0.f;
#pragma unroll
    for (int i = 0; i < 16; ++i) { const float d = v[i] - mean; q = fmaf(d, d, q); }
    const float var = wave_sum(q) * (1.f / DM);
    const float mb = rbf(mean), vb_ = rbf(var);
    const float den = rbf(sqrtf(rbf(vb_ + 1e-5f)));
    const float rd = frcp(den);
    WAITG(0); UPTO(0);
    u32x4 o0, o1;
#pragma unroll
    for (int i = 0; i < 8; ++i) {
      const float a0 = rbf(v[2 * i] - mb), a1 = rbf(v[2 * i + 1] - mb);
      float q0 = a0 * rd, q1 = a1 * rd;
      q0 = fmaf(fmaf(-q0, den, a0), rd, q0); q1 = fmaf(fmaf(-q1, den, a1), rd, q1);
      if (i < 4) o0[i] = cvt_pk(q0, q1); else o1[i - 4] = cvt_pk(q0, q1);
    }
    asm volatile("s_waitcnt lgkmcnt(0)" ::"s"(tw0), "s"(tw1), "s"(tw2), "s"(tw3), "s"(tw4));
    char* d = lds + L_XN + wave * 2064 + lane * 32;
    *(u32x4*)d = o0; *(u32x4*)(d + 16) = o1;
  }
  wg_barrier();
  const u32x4 xq = *(const u32x4*)(lds + L_XN + (c & 3) * 2064 + xcd * 256 + wave * 64 + g * 16);
  const u64 epoch = (((u64)(u32)__builtin_amdgcn_readfirstlane(e2[1])) << 32 | (u32)__builtin_amdgcn_readfirstlane(e2[0])) + 1ull;
  const u32 T12 = (u32)epoch & 0xfffu;
  const u32 QTAG = ((u32)epoch & 0xfffffu) << 12;
  __builtin_amdgcn_raw_buffer_store_b32(QTAG, rs_ws, (bid == 0 && tid < NSH) ? WS_CF + tid * 256 : OOB, 0, 16);

  WAITG(1); UPTO(1);
  {
    f32x4 acc[8];
#pragma unroll
    for (int i = 0; i < 8; ++i) acc[i] = mfma(xq, colfrag(wq, i), f32x4{0.f, 0.f, 0.f, 0.f});
    if (g == 0) {
      char* d = lds + L_REDQ + wave * 2048;
#pragma unroll
      for (int b = 0; b < 4; ++b) {
        *(f32x4*)(d + b * 512 + ((2 * c) ^ (c >> 2 & 1)) * 16) = f32x4{acc[0][b], acc[1][b], acc[2][b], acc[3][b]};
        *(f32x4*)(d + b * 512 + ((2 * c + 1) ^ (c >> 2 & 1)) * 16) = f32x4{acc[4][b], acc[5][b], acc[6][b], acc[7][b]};
      }
    }
  }
  wg_barrier();
  {
    const int t7 = tid & 127;
    const char* s = lds + L_REDQ + (t7 ^ (t7 >> 3 & 1)) * 16;
    const f32x4 a = *(const f32x4*)s + *(const f32x4*)(s + 2048) + *(const f32x4*)(s + 4096) + *(const f32x4*)(s + 6144);
    const u32x4 au = __builtin_bit_cast(u32x4, a);
    const u32 xr = half_xor(au[0] ^ au[1] ^ au[2] ^ au[3]);
    st_sc(au, rs_ws, tid < 128 ? WS_QP + (u32)((xcd * 4 + (t7 >> 5)) * 4096 + r * 128 + (t7 & 31) * 4) * 4 : OOB, 0);
    st_sc(u32x4{(u32)epoch, (u32)(epoch >> 32), xr, 0u}, rs_ws, (tid < 128 && (tid & 31) == 0) ? WS_QC + (u32)(((t7 >> 5) * 32 + r) * 8 + xcd) * 16 : OOB, 0);
  }

  char* kl = lds + L_K + wave * 32768;
#define KSTORE(n)                                                                                   \
  _Pragma("unroll") for (int j = 0; j < 4; ++j) {                                                   \
    const int t_ = ((n) - 2) * 4 + j;                                                               \
    *(u32x4*)(kl + (t_ * 4 + g) * 256 + ((c ^ (g * 4 + j)) * 16)) = kb[t_];                         \
  }
  u32x4 qp[8], qc;
  const u32 qp_off = WS_QP + (u32)(b_att * 4096 + kvh * 1024 + tid * 4) * 4;
  const u32 qc_off = WS_QC + (u32)((b_att * 32 + kvh * 8 + (tid >> 5)) * 8 + (tid & 7)) * 16;
  WAITG(2); UPTO(2); KSTORE(2)
  WAITG(3); UPTO(3); KSTORE(3)
  WAITG(4); UPTO(4); KSTORE(4)
  WAITG(5); UPTO(5); KSTORE(5)
  WAITG(6); SB(); qp[0] = ld_sc(rs_ws, qp_off, 0); qp[1] = ld_sc(rs_ws, qp_off, 65536); UPTO(6); KSTORE(6)
  WAITG(7); SB(); qp[2] = ld_sc(rs_ws, qp_off, 2 * 65536); qp[3] = ld_sc(rs_ws, qp_off, 3 * 65536); UPTO(7); KSTORE(7)
  WAITG(8); SB(); qp[4] = ld_sc(rs_ws, qp_off, 4 * 65536); qp[5] = ld_sc(rs_ws, qp_off, 5 * 65536); UPTO(8); KSTORE(8)
  WAITG(9); SB(); qp[6] = ld_sc(rs_ws, qp_off, 6 * 65536); qp[7] = ld_sc(rs_ws, qp_off, 7 * 65536); qc = ld_sc(rs_ws, qc_off, 0); UPTO(9); KSTORE(9)
  WAITG(10); UPTO(10);
#define QBAD(bad)                                                                                   \
  {                                                                                                 \
    u32 x_ = (tid & 31) < 8 ? qc[2] : 0u;                                                           \
    _Pragma("unroll") for (int s = 0; s < 8; ++s) x_ ^= qp[s][0] ^ qp[s][1] ^ qp[s][2] ^ qp[s][3];  \
    bad = half_xor(x_) | (qc[0] ^ (u32)epoch) | (qc[1] ^ (u32)(epoch >> 32));                       \
  }
  {
    u32 bad;
    QBAD(bad)
    if (!__all(bad == 0)) {
      for (int spins = 0; spins < SPIN_CAP; ++spins) {
        asm volatile("" ::: "memory");
#pragma unroll
        for (int s = 0; s < 8; ++s) qp[s] = ld_sc(rs_ws, qp_off, s * 65536);
        qc = ld_sc(rs_ws, qc_off, 0);
        QBAD(bad)
        if (__all(bad == 0)) break;
      }
    }
    f32x4 a = __builtin_bit_cast(f32x4, qp[0]);
#pragma unroll
    for (int s = 1; s < 8; ++s) a += __builtin_bit_cast(f32x4, qp[s]);
    *(u32x2*)(lds + L_QS + (tid >> 5) * 272 + (tid & 31) * 8) = u32x2{cvt_pk(a[0], a[1]), cvt_pk(a[2], a[3])};
  }
  wg_barrier();

  u32x4 pf[2];
  float m_w, l_w;
  {
    u32x4 qlo[4], qhi[4];
#pragma unroll
    for (int s = 0; s < 4; ++s) {
      const u32x4 qv = *(const u32x4*)(lds + L_QS + (c & 7) * 272 + (4 * s + g) * 16);
      const u32x4 z = {0u, 0u, 0u, 0u};
      qlo[s] = c < 8 ? qv : z; qhi[s] = c < 8 ? z : qv;
    }
    const int rho = (c & 3) * 4 + (c >> 2);
    float sc[4][4];
#pragma unroll
    for (int u = 0; u < 2; ++u) {
#pragma unroll
      for (int a = 0; a < 2; ++a) {
        f32x4 acc = {0.f, 0.f, 0.f, 0.f};
#pragma unroll
        for (int s = 0; s < 4; ++s) {
          const u32x4 kf = *(const u32x4*)(kl + ((u * 4 + a) * 16 + rho) * 256 + (((4 * s + g) ^ c) * 16));
          acc = mfma(kf, qlo[s], acc);
        }
#pragma unroll
        for (int s = 0; s < 4; ++s) {
          const u32x4 kf = *(const u32x4*)(kl + ((u * 4 + 2 + a) * 16 + rho) * 256 + (((4 * s + g) ^ c) * 16));
          acc = mfma(kf, qhi[s], acc);
        }
        const u32 p01 = cvt_pk(acc[0], acc[1]), p23 = cvt_pk(acc[2], acc[3]);
        const u32 r01 = cvt_pk(bf_lo(p01) * 0.08838834764831845f, bf_hi(p01) * 0.08838834764831845f);
        const u32 r23 = cvt_pk(bf_lo(p23) * 0.08838834764831845f, bf_hi(p23) * 0.08838834764831845f);
        sc[u * 2 + a][0] = bf_lo(r01); sc[u * 2 + a][1] = bf_hi(r01); sc[u * 2 + a][2] = bf_lo(r23); sc[u * 2 + a][3] = bf_hi(r23);
      }
      if (u == 0) { WAITG(11); UPTO(11); }
    }
    float m = sc[0][0];
#pragma unroll
    for (int a = 0; a < 4; ++a)
#pragma unroll
      for (int i = 0; i < 4; ++i) m = fmaxf(m, sc[a][i]);
    m = fmaxf(m, dpp<XOR8>(m));
    m = xrow_max(m);
    const float ml2 = m * 1.4426950408889634f;
    float l = 0.f;
#pragma unroll
    for (int a = 0; a < 4; ++a) {
#pragma unroll
      for (int i = 0; i < 4; ++i) { sc[a][i] = __builtin_amdgcn_exp2f(fmaf(sc[a][i], 1.4426950408889634f, -ml2)); l += sc[a][i]; }
    }
#pragma unroll
    for (int u = 0; u < 2; ++u) {
      pf[u][0] = cvt_pk(sc[2 * u][0], sc[2 * u][1]);
      pf[u][1] = cvt_pk(sc[2 * u][2], sc[2 * u][3]);
      pf[u][2] = cvt_pk(sc[2 * u + 1][0], sc[2 * u + 1][1]);
      pf[u][3] = cvt_pk(sc[2 * u + 1][2], sc[2 * u + 1][3]);
    }
    l += dpp<XOR8>(l);
    m_w = m; l_w = xrow_sum(l);
  }

  f32x4 oa[8];
  u32x4 pl[2], ph[2];
  {
    const u32x4 z = {0u, 0u, 0u, 0u};
#pragma unroll
    for (int u = 0; u < 2; ++u) { pl[u] = c < 8 ? pf[u] : z; ph[u] = c < 8 ? z : pf[u]; }
  }
  WAITG(12); UPTO(12);
#pragma unroll
  for (int i = 0; i < 8; ++i) oa[i] = mfma(colfrag(vb[0], i), pl[0], f32x4{0.f, 0.f, 0.f, 0.f});
  WAITG(13); UPTO(13);
#pragma unroll
  for (int i = 0; i < 8; ++i) oa[i] = mfma(colfrag(vb[1], i), ph[0], oa[i]);
  WAITG(14); SB();
  u32 clm = at_add(1u, rs_ws, tid == 0 ? WS_CF + shard * 256 : OOB);
  UPTO(14);
  WAITG(15); UPTO(15);
#pragma unroll
  for (int i = 0; i < 8; ++i) oa[i] = mfma(colfrag(vb[2], i), pl[1], oa[i]);
  WAITG(16);
  if (tid == 0 && (clm & 0xfffff000u) != QTAG) {
    for (int spins = 0; spins < SPIN_CAP; ++spins) {
      asm volatile("" ::: "memory");
      if ((__builtin_amdgcn_raw_buffer_load_b32(rs_ws, WS_CF + shard * 256, 0, 16) & 0xfffff000u) == QTAG) break;
    }
    clm = at_add(1u, rs_ws, WS_CF + shard * 256);
  }
  if (tid == 0) *(u32*)(lds + L_CL) = min(clm & 0xfffu, (u32)(256 / NSH - 1));
  wg_barrier();
  const int kq = __builtin_amdgcn_readfirstlane(*(const u32*)(lds + L_CL));
  constexpr int PF = 224 / NSH, PS = 32 / NSH;
  const bool has_f = kq < PF, has_d = kq < PF - PS || kq >= PF;
  const int tfc = has_f ? shard * PF + kq : 0;
  const int kgf = tfc / 28, cgf = tfc - kgf * 28;
  rs_wf = mk(((kgf & 1) ? p.wu : p.wg) + (size_t)(256 * (kgf >> 1)) * 7168 + cgf * 256, has_f ? 256 * 7168 - cgf * 256 : 0);
  const int tdn = shard * PF + (kq >= PF ? kq - PF : (has_d ? kq + PS : 0));
  const int rgd = tdn >> 3, cgd = tdn & 7;
  rs_wd = mk(p.wd + (size_t)rgd * 128 * 2048 + cgd * 256, has_d ? 128 * 2048 - cgd * 256 : 0);
  const rsrc_t rs_wsd = mk(p.ws, has_d ? WS_END : 0u);
  UPTO(16);
  WAITG(17); UPTO(17);
#pragma unroll
  for (int i = 0; i < 8; ++i) oa[i] = mfma(colfrag(vb[3], i), ph[1], oa[i]);
  {
    char* d = kl + LK_MRG + c * 512 + g * 128;
#pragma unroll
    for (int i = 0; i < 8; ++i) *(f32x4*)(d + (i ^ (c & 7)) * 16) = oa[i];
    if (lane < 8) *(float2*)(lds + L_ML + (wave * 8 + c) * 8) = make_float2(m_w, l_w);
  }
  wg_barrier();
  {
    const int hh = tid >> 5, d4 = tid & 31;
    float2 ml[4];
#pragma unroll
    for (int w = 0; w < 4; ++w) ml[w] = *(const float2*)(lds + L_ML + (w * 8 + hh) * 8);
    const float M = fmaxf(fmaxf(ml[0].x, ml[1].x), fmaxf(ml[2].x, ml[3].x));
    f32x4 o = {0.f, 0.f, 0.f, 0.f};
    float L = 0.f;
#pragma unroll
    for (int w = 0; w < 4; ++w) {
      const float f = fexp(ml[w].x - M);
      L = fmaf(f, ml[w].y, L);
      const char* s = lds + L_K + w * 32768 + LK_MRG + hh * 512 + (d4 ^ hh) * 16;
      o += f * (*(const f32x4*)s + *(const f32x4*)(s + 4096));
    }
    o *= frcp(L);
    const u32 p0 = cvt_pk(o[0], o[1]), p1 = cvt_pk(o[2], o[3]);
    const u32x4 pk = {p0, p1, dppu<XOR1>(p0), dppu<XOR1>(p1)};
    const u32 xr = half_xor(p0 ^ p1) ^ __float_as_uint(M) ^ __float_as_uint(L) ^ (u32)(epoch >> 32);
    const u32 slot = (u32)((b_att * 32 + kvh * 8 + hh) * 16 + split);
    st_sc(pk, rs_ws, (d4 & 1) == 0 ? WS_OP + slot * 256 + d4 * 8 : OOB, 0);
    st_sc(u32x4{__float_as_uint(M), __float_as_uint(L), xr, (u32)epoch}, rs_ws, d4 == 0 ? WS_ML + slot * 16 : OOB, 0);
  }

  u32x4 og[4], mlv;
  const u32 og_off = WS_OP + (u32)((wave * 32 + rgo) * 16 + g * 4) * 256 + c * 16;
  const u32 ml_off = WS_ML + (u32)((wave * 32 + rgo) * 16 + ((c + g * 4) & 15)) * 16;
  {
    f32x4 acc[2] = {{0.f, 0.f, 0.f, 0.f}, {0.f, 0.f, 0.f, 0.f}};
    const u32x4 xfa = *(const u32x4*)(lds + L_XN + (c & 3) * 2064 + (kgf >> 1) * 512 + wave * 64 + g * 16);
    const u32x4 xfb = *(const u32x4*)(lds + L_XN + (c & 3) * 2064 + (kgf >> 1) * 512 + 256 + wave * 64 + g * 16);
    u32x4 ama[4], amb[4];
    amask(xfa, c, ama); amask(xfb, c, amb);
    WAITG(18); UPTO(18);
    WAITG(19); UPTO(19);
#pragma unroll
    for (int i = 0; i < 8; ++i) acc[i >> 2] = mfma(ama[i & 3], colfrag(fa, i), acc[i >> 2]);
    WAITG(20); UPTO(20);
    WAITG(21); SB();
#pragma unroll
    for (int i = 0; i < 8; ++i) acc[i >> 2] = mfma(amb[i & 3], colfrag(fb, i), acc[i >> 2]);
    tile_store2(kl + LK_RF, acc, c, g);
  }
  wg_barrier();
  {
    const char* s = lds + L_K + LK_RF + tile_off(tid & 127);
    f32x4 a = (*(const f32x4*)s + *(const f32x4*)(s + 32768)) + (*(const f32x4*)(s + 65536) + *(const f32x4*)(s + 98304));
    st_sc(tag4(a, T12), rs_ws, (has_f && tid < 128) ? WS_GU + (u32)(kgf * DF + cgf * 128 + tid) * 16 : OOB, 0);
  }
  SB();
#pragma unroll
  for (int s = 0; s < 4; ++s) og[s] = ld_sc(rs_ws, og_off, s * 256);
  mlv = ld_sc(rs_ws, ml_off, 0);
  UPTO(21);
  SB();
  WAITG(22); UPTO(22);
#define OBAD(bad)                                                                                                  \
  {                                                                                                                \
    const u32 want = mlv[2] ^ mlv[0] ^ mlv[1] ^ (u32)(epoch >> 32);              \
    u32 x_ = c < 4 ? want : 0u;                                         \
    _Pragma("unroll") for (int s = 0; s < 4; ++s) x_ ^= og[s][0] ^ og[s][1] ^ og[s][2] ^ og[s][3];                 \
    bad = row_xor(x_) | (mlv[3] ^ (u32)epoch);                                                                     \
  }
  {
    u32 bad;
    OBAD(bad)
    if (!__all(bad == 0)) {
      for (int spins = 0; spins < SPIN_CAP; ++spins) {
        asm volatile("" ::: "memory");
#pragma unroll
        for (int s = 0; s < 4; ++s) og[s] = ld_sc(rs_ws, og_off, s * 256);
        mlv = ld_sc(rs_ws, ml_off, 0);
        OBAD(bad)
        if (__all(bad == 0)) break;
      }
    }
  }
  {
    const float ms = __uint_as_float(mlv[0]), ls = __uint_as_float(mlv[1]);
    const float M = row16_max(ms);
    const float wl = fexp(ms - M) * ls;
    const float w = wl * frcp(row16_sum(wl));
    const float w0 = dpp<0x150>(w), w1 = dpp<0x151>(w), w2 = dpp<0x152>(w), w3 = dpp<0x153>(w);
    u32x4 pk;
#pragma unroll
    for (int i = 0; i < 4; ++i) {
      float lo = w0 * bf_lo(og[0][i]), hi = w0 * bf_hi(og[0][i]);
      lo = fmaf(w1, bf_lo(og[1][i]), lo); hi = fmaf(w1, bf_hi(og[1][i]), hi);
      lo = fmaf(w2, bf_lo(og[2][i]), lo); hi = fmaf(w2, bf_hi(og[2][i]), hi);
      lo = fmaf(w3, bf_lo(og[3][i]), lo); hi = fmaf(w3, bf_hi(og[3][i]), hi);
      pk[i] = cvt_pk(xrow_sum(lo), xrow_sum(hi));
    }
    if (lane < 16) *(u32x4*)(lds + L_OS + wave * 256 + lane * 16) = pk;
  }
  WAITG(23);
  u32x4 gp[4];
  const u32 gu_off = WS_GU + (u32)((lane >> 5) * DF + rgd * 128 + wave * 32 + (lane & 31)) * 16;
  asm volatile("" ::: "memory");
#pragma unroll
  for (int s = 0; s < 4; ++s) gp[s] = ld_sc(rs_wsd, gu_off, s * (2 * DF * 16));
  SB();
  UPTO(23);
  u32x4 of_[8];
#pragma unroll
  for (int i = 0; i < 8; ++i) of_[i] = colfrag(wo, i);
  wg_barrier();
  {
    const u32x4 of = *(const u32x4*)(lds + L_OS + (c & 3) * 256 + wave * 64 + g * 16);
    f32x4 acc[2] = {{0.f, 0.f, 0.f, 0.f}, {0.f, 0.f, 0.f, 0.f}};
    u32x4 am[4];
    amask(of, c, am);
#pragma unroll
    for (int i = 0; i < 8; ++i) acc[i & 1] = mfma(am[i >> 1], of_[(i & 1) * 4 + (i >> 1)], acc[i & 1]);
    tile_store2(kl + LK_RO, acc, c, g);
  }
  wg_barrier();
  {
    const char* s = lds + L_K + LK_RO + tile_off(tid & 127);
    f32x4 a = (*(const f32x4*)s + *(const f32x4*)(s + 32768)) + (*(const f32x4*)(s + 65536) + *(const f32x4*)(s + 98304));
    st_sc(tag4(a, T12), rs_ws, tid < 128 ? WS_FP + (u32)(rgo * 1024 + cgo * 128 + tid) * 16 : OOB, 0);
  }
  SB();
  {
    u32 bad = 0;
#pragma unroll
    for (int s = 0; s < 4; ++s) bad |= tagbad(gp[s], T12);
    if (has_d && !__all(bad == 0)) {
      for (int spins = 0; spins < SPIN_CAP; ++spins) {
        asm volatile("" ::: "memory");
#pragma unroll
        for (int s = 0; s < 4; ++s) gp[s] = ld_sc(rs_wsd, gu_off, s * (2 * DF * 16));
        bad = 0;
#pragma unroll
        for (int s = 0; s < 4; ++s) bad |= tagbad(gp[s], T12);
        if (__all(bad == 0)) break;
      }
    }
    const f32x4 a = (untag(gp[0]) + untag(gp[1])) + (untag(gp[2]) + untag(gp[3]));
#pragma unroll
    for (int b = 0; b < 4; ++b) {
      const float v = rbf(a[b]);
      auto t = __builtin_amdgcn_permlane32_swap(__float_as_uint(v), __float_as_uint(v), false, false);
      const float gv = __uint_as_float(t[0]), uv = __uint_as_float(t[1]);
      const float inner = 0.7978845608028654f * (gv + 0.044715f * gv * gv * gv);
      const float th = 1.f - 2.f * frcp(fexp(2.f * inner) + 1.f);
      const float ge = rbf(0.5f * gv * (1.f + th));
      const u32 hv = cvt_pk(ge * uv, 0.f);
      if (lane < 32) *(unsigned short*)(kl + LK_HS + b * 64 + lane * 2) = (unsigned short)hv;
    }
  }
  asm volatile("s_waitcnt lgkmcnt(0)" ::: "memory");
  SB();
  u32x4 df[8];
#pragma unroll
  for (int i = 0; i < 8; ++i) df[i] = colfrag(wd, i);
  {
    const u32x4 hf = *(const u32x4*)(kl + LK_HS + (c & 3) * 64 + g * 16);
    f32x4 acc[2] = {{0.f, 0.f, 0.f, 0.f}, {0.f, 0.f, 0.f, 0.f}};
    u32x4 am[4];
    amask(hf, c, am);
#pragma unroll
    for (int i = 0; i < 8; ++i) acc[i & 1] = mfma(am[i >> 1], df[(i & 1) * 4 + (i >> 1)], acc[i & 1]);
    tile_store2(kl + LK_RD, acc, c, g);
  }
  wg_barrier();
  {
    const char* s = lds + L_K + LK_RD + tile_off(tid & 127);
    f32x4 a = (*(const f32x4*)s + *(const f32x4*)(s + 32768)) + (*(const f32x4*)(s + 65536) + *(const f32x4*)(s + 98304));
    st_sc(tag4(a, T12), rs_wsd, tid < 128 ? WS_FP + (u32)((32 + rgd) * 1024 + cgd * 128 + tid) * 16 : OOB, 0);
  }
  SB();
  {
    const int col0 = (xcd * 32 + r) * 4;
    const int ks = tid >> 2, cc = tid & 3;
    const bool act = ks < 60;
    const u32 off = act ? WS_FP + (u32)(ks * 1024 + col0 + cc) * 16 : OOB;
    const unsigned short xin = *(const unsigned short*)(p.x + (((tid >> 2) & 3) * DM + col0 + cc) * 2);
    u32x4 fv;
    for (int spins = 0; spins < SPIN_CAP; ++spins) {
      fv = ld_sc(rs_ws, off, 0);
      const u32 bad = act ? tagbad(fv, T12) : 0u;
      if (__all(bad == 0)) break;
      asm volatile("" ::: "memory");
    }
    f32x4 a = untag(fv);
#pragma unroll
    for (int i = 0; i < 4; ++i) {
      float t = a[i];
      t += dpp<0x124>(t);
      t += dpp<XOR8>(t);
      a[i] = xrow_sum(t);
    }
    if (lane < 4) *(f32x4*)(lds + L_K + LK_FIN + (wave * 4 + lane) * 16) = a;
    wg_barrier();
    if (tid < 16) {
      const int b = tid >> 2, cc2 = tid & 3;
      const float* fs = (const float*)(lds + L_K + LK_FIN) + cc2 * 4 + b;
      const float s = rbf(fs[0] + fs[16]);
      const float dd = rbf(fs[32] + fs[48]);
      const float gt = rbf(dd * frcp(1.f + fexp(-dd)));
      const float o = bf_lo(xin) + rbf(s * gt);
      *(unsigned short*)(p.out + (b * DM + col0 + cc2) * 2) = (unsigned short)cvt_pk(o, o);
    }
    if (bid == 0 && tid == 0) st_sc64(u32x2{(u32)epoch, (u32)(epoch >> 32)}, rs_ws, WS_E, 0);
  }
  KEEP(cw0);
  asm volatile(".Lfdk_end:");
}

extern "C" void kernel_launch(void* const* d_in, const int* in_sizes, int n_in, void* d_out, int out_size, void* d_ws,
                              size_t ws_size, hipStream_t stream) {
  Params p;
  p.x = (const char*)d_in[0]; p.k = (const char*)d_in[1]; p.v = (const char*)d_in[2];
  p.wq = (const char*)d_in[3]; p.wo = (const char*)d_in[4];
  p.wg = (const char*)d_in[6]; p.wu = (const char*)d_in[7]; p.wd = (const char*)d_in[8];
  p.out = (char*)d_out; p.ws = (char*)d_ws;
  (void)hipFuncSetAttribute((const void*)fused_decode_kernel, hipFuncAttributeMaxDynamicSharedMemorySize, L_END);
  fused_decode_kernel<<<NWG, NTHR, L_END, stream>>>(p);
}
```

```cpp
#include <hip/hip_runtime.h>
#include <stdint.h>

typedef unsigned u32;
typedef unsigned long long u64;
typedef u32 u32x4 __attribute__((ext_vector_type(4)));
typedef u32 u32x2 __attribute__((ext_vector_type(2)));
typedef float f32x4 __attribute__((ext_vector_type(4)));
typedef __bf16 bf16x8 __attribute__((ext_vector_type(8)));
typedef __amdgpu_buffer_rsrc_t rsrc_t;

constexpr int NB = 4, DM = 1024, SK = 8192, DF = 3584;
constexpr int NWG = 256, NTHR = 256;

constexpr u32 WS_E = 0;
constexpr u32 WS_CF = 1024;
constexpr u32 WS_QC = 4096;
constexpr u32 WS_QP = 32768;
constexpr u32 WS_GU = WS_QP + 8 * 4 * 4096 * 4;
constexpr u32 WS_OP = WS_GU + 4 * 2 * DF * 16;
constexpr u32 WS_ML = WS_OP + 4 * 32 * 16 * 256;
constexpr u32 WS_FP = WS_ML + 4 * 32 * 16 * 16;
constexpr u32 WS_END = WS_FP + 60 * 1024 * 16;

constexpr int L_K = 0;
constexpr int L_REDQ = 131072;
constexpr int L_XN = L_REDQ + 8192;
constexpr int L_QS = L_XN + 4 * 2064;
constexpr int L_OS = L_QS + 8 * 272;
constexpr int L_ML = L_OS + 1024;
constexpr int L_CL = L_ML + 256;
constexpr int L_END = L_CL + 16;
constexpr int LK_MRG = 0;
constexpr int LK_RF = 8192;
constexpr int LK_RD = 10240;
constexpr int LK_RO = 12288;
constexpr int LK_HS = 14336;
constexpr int LK_FIN = 16384;

struct Params {
  const char* x; const char* k; const char* v; const char* wq; const char* wo;
  const char* wg; const char* wu; const char* wd; char* out; char* ws;
};

#define SB() __builtin_amdgcn_sched_barrier(0)
#define KEEP(x) do { SB(); asm volatile("" ::"v"(x)); } while (0)

__device__ __forceinline__ rsrc_t mk(const char* p, u32 bytes) {
  return __builtin_amdgcn_make_buffer_rsrc((void*)p, 0, bytes, 0x00020000);
}
__device__ __forceinline__ u32x4 ld_nt(rsrc_t r, u32 voff, u32 soff) { return __builtin_amdgcn_raw_buffer_load_b128(r, voff, soff, 2); }
__device__ __forceinline__ u32x4 ld_pl(rsrc_t r, u32 voff, u32 soff) { return __builtin_amdgcn_raw_buffer_load_b128(r, voff, soff, 0); }
__device__ __forceinline__ u32x4 ld_sc(rsrc_t r, u32 voff, u32 soff) { return __builtin_amdgcn_raw_buffer_load_b128(r, voff, soff, 16); }
__device__ __forceinline__ u32x2 ld_sc64(rsrc_t r, u32 voff, u32 soff) { return __builtin_amdgcn_raw_buffer_load_b64(r, voff, soff, 16); }
__device__ __forceinline__ void st_sc(u32x4 v, rsrc_t r, u32 voff, u32 soff) { __builtin_amdgcn_raw_buffer_store_b128(v, r, voff, soff, 16); }
__device__ __forceinline__ void st_sc64(u32x2 v, rsrc_t r, u32 voff, u32 soff) { __builtin_amdgcn_raw_buffer_store_b64(v, r, voff, soff, 16); }

__device__ __forceinline__ void wg_barrier() {
  asm volatile("s_waitcnt lgkmcnt(0)\n\ts_barrier" ::: "memory");
}
__device__ __forceinline__ float bf_lo(u32 v) { return __uint_as_float(v << 16); }
__device__ __forceinline__ float bf_hi(u32 v) { return __uint_as_float(v & 0xffff0000u); }
typedef __bf16 bf16x2 __attribute__((ext_vector_type(2)));
typedef float f32x2 __attribute__((ext_vector_type(2)));
__device__ __forceinline__ u32 cvt_pk(float lo, float hi) {
  return __builtin_bit_cast(u32, __builtin_convertvector(f32x2{lo, hi}, bf16x2));
}
__device__ __forceinline__ float rbf(float f) { return bf_lo(cvt_pk(f, f)); }

template <int CTRL> __device__ __forceinline__ float dpp(float x) {
  return __builtin_bit_cast(float, __builtin_amdgcn_mov_dpp(__builtin_bit_cast(int, x), CTRL, 0xf, 0xf, true));
}
constexpr int XOR1 = 0xB1, XOR2 = 0x4E, XOR7 = 0x141, XOR8 = 0x128;
__device__ __forceinline__ float xrow_sum(float x) {
  auto s = __builtin_amdgcn_permlane16_swap(__float_as_uint(x), __float_as_uint(x), false, false);
  x = __uint_as_float(s[0]) + __uint_as_float(s[1]);
  auto t = __builtin_amdgcn_permlane32_swap(__float_as_uint(x), __float_as_uint(x), false, false);
  return __uint_as_float(t[0]) + __uint_as_float(t[1]);
}
__device__ __forceinline__ float xrow_max(float x) {
  auto s = __builtin_amdgcn_permlane16_swap(__float_as_uint(x), __float_as_uint(x), false, false);
  x = fmaxf(__uint_as_float(s[0]), __uint_as_float(s[1]));
  auto t = __builtin_amdgcn_permlane32_swap(__float_as_uint(x), __float_as_uint(x), false, false);
  return fmaxf(__uint_as_float(t[0]), __uint_as_float(t[1]));
}
__device__ __forceinline__ float wave_sum(float x) {
  x += dpp<XOR1>(x); x += dpp<XOR2>(x); x += dpp<XOR7>(x); x += dpp<XOR8>(x);
  return xrow_sum(x);
}
__device__ __forceinline__ float row16_max(float x) {
  x = fmaxf(x, dpp<XOR1>(x)); x = fmaxf(x, dpp<XOR2>(x)); x = fmaxf(x, dpp<XOR7>(x)); x = fmaxf(x, dpp<XOR8>(x));
  return x;
}
__device__ __forceinline__ float row16_sum(float x) {
  x += dpp<XOR1>(x); x += dpp<XOR2>(x); x += dpp<XOR7>(x); x += dpp<XOR8>(x);
  return x;
}
__device__ __forceinline__ float fexp(float x) { return __builtin_amdgcn_exp2f(x * 1.4426950408889634f); }

__device__ __forceinline__ f32x4 mfma(u32x4 a, u32x4 b, f32x4 c) {
  return __builtin_amdgcn_mfma_f32_16x16x32_bf16(__builtin_bit_cast(bf16x8, a), __builtin_bit_cast(bf16x8, b), c, 0, 0, 0);
}
__device__ __forceinline__ u32x4 colfrag(const u32x4 (&w)[8], int i) {
  const u32 sel = (i & 1) ? 0x07060302u : 0x05040100u;
  const int d = i >> 1;
  u32x4 f;
  f[0] = __builtin_amdgcn_perm(w[1][d], w[0][d], sel);
  f[1] = __builtin_amdgcn_perm(w[3][d], w[2][d], sel);
  f[2] = __builtin_amdgcn_perm(w[5][d], w[4][d], sel);
  f[3] = __builtin_amdgcn_perm(w[7][d], w[6][d], sel);
  return f;
}
__device__ __forceinline__ u32 tag1(float v, u32 t) { return ((__float_as_uint(v) + 0x800u) & 0xfffff000u) | t; }
__device__ __forceinline__ u32x4 tag4(f32x4 v, u32 t) { u32x4 r = {tag1(v[0], t), tag1(v[1], t), tag1(v[2], t), tag1(v[3], t)}; return r; }
__device__ __forceinline__ u32 tagbad(u32x4 v, u32 t) { return ((v[0] ^ t) | (v[1] ^ t) | (v[2] ^ t) | (v[3] ^ t)) & 0xfffu; }
__device__ __forceinline__ f32x4 untag(u32x4 v) {
  f32x4 r = {__uint_as_float(v[0] & 0xfffff000u), __uint_as_float(v[1] & 0xfffff000u), __uint_as_float(v[2] & 0xfffff000u), __uint_as_float(v[3] & 0xfffff000u)};
  return r;
}
constexpr int SPIN_CAP = 1 << 18;
constexpr int NSH = 4;
__device__ __forceinline__ u32 at_add(u32 v, rsrc_t r, u32 voff) {
  return (u32)__builtin_amdgcn_raw_ptr_buffer_atomic_add_i32((int)v, r, voff, 0, 0);
}
constexpr u32 OOB = 0x80000000u;
template <int CTRL> __device__ __forceinline__ u32 dppu(u32 x) { return (u32)__builtin_amdgcn_mov_dpp((int)x, CTRL, 0xf, 0xf, true); }
__device__ __forceinline__ u32 csum4(u32x4 v) { return v[0] + v[1] + v[2] + v[3] + v[3]; }
__device__ __forceinline__ u32 row_add(u32 x) {
  x += dppu<XOR1>(x); x += dppu<XOR2>(x); x += dppu<XOR7>(x); x += dppu<XOR8>(x);
  return x;
}
__device__ __forceinline__ u32 half_add(u32 x) {
  x = row_add(x);
  auto t = __builtin_amdgcn_permlane16_swap(x, x, false, false);
  return t[0] + t[1];
}
__device__ __forceinline__ float frcp(float x) { return __builtin_amdgcn_rcpf(x); }
__device__ __forceinline__ void amask(u32x4 a, int c, u32x4 (&am)[4]) {
  const u32x4 z = {0u, 0u, 0u, 0u};
#pragma unroll
  for (int q = 0; q < 4; ++q) am[q] = (c >> 2) == q ? a : z;
}
__device__ __forceinline__ void tile_store2(char* d, const f32x4 (&acc)[2], int c, int g) {
#pragma unroll
  for (int j = 0; j < 2; ++j) *(f32x4*)(d + (c * 8 + ((j * 4 + g) ^ (c & 7))) * 16) = acc[j];
}
__device__ __forceinline__ int tile_off(int col) { return (col ^ ((col >> 3) & 7)) * 16; }

__global__ void __launch_bounds__(NTHR, 1) fused_decode_kernel(Params p) {
  extern __shared__ __attribute__((aligned(16))) char lds[];
  const int tid = threadIdx.x, lane = tid & 63;
  const int wave = __builtin_amdgcn_readfirstlane(tid >> 6);
  const int c = lane & 15, g = lane >> 4;
  const int bid = blockIdx.x, xcd = bid & 7, r = bid >> 3;

  u32 cw0;
  {
    u64 pc; u32 len;
    asm volatile("s_getpc_b64 %0\n.Lfdk_here:\n\ts_mov_b32 %1, .Lfdk_end-.Lfdk_here" : "=s"(pc), "=s"(len));
    len = (int)len > 0 ? len : 0u;
    const rsrc_t rs_c = mk((const char*)pc, len);
    cw0 = __builtin_amdgcn_raw_buffer_load_b32(rs_c, tid * 128, 0, 0);
  }
  SB();
  const int pair = xcd * 2 + (r >> 4), b_att = pair >> 2, kvh = pair & 3, split = r & 15;
  const rsrc_t rs_ws = mk(p.ws, WS_END);
  const rsrc_t rs_x = mk(p.x, NB * DM * 2);
  const rsrc_t rs_wq = mk(p.wq + (size_t)(128 * xcd) * 8192 + r * 256, 128 * 8192 - r * 256);
  const rsrc_t rs_k = mk(p.k + ((size_t)pair * SK + split * 512) * 256, 512 * 256);

  u32 tw0, tw1, tw2, tw3, tw4;
  {
    const char* a0 = p.wq + (size_t)(128 * xcd) * 8192 + r * 256;
    const char* a1 = p.k + ((size_t)pair * SK + split * 512) * 256;
    const char* a2 = p.x;
    const char* a3 = p.wq + (size_t)(128 * xcd + 127) * 8192 + r * 256;
    const char* a4 = p.ws;
    asm volatile("s_load_dword %0, %5, 0x0\n\ts_load_dword %1, %6, 0x0\n\ts_load_dword %2, %7, 0x0\n\ts_load_dword %3, %8, 0x0\n\ts_load_dword %4, %9, 0x0"
                 : "=&s"(tw0), "=&s"(tw1), "=&s"(tw2), "=&s"(tw3), "=&s"(tw4) : "s"(a0), "s"(a1), "s"(a2), "s"(a3), "s"(a4));
  }
  u32x2 e2 = ld_sc64(rs_ws, WS_E, 0);
  u32x4 x0 = ld_pl(rs_x, wave * 2048 + lane * 32, 0);
  u32x4 x1 = ld_pl(rs_x, wave * 2048 + lane * 32 + 16, 0);
  SB();
  u32x4 wq[8], kb[32], vb[4][8], fa[8], fb[8], wo[8], wd[8];
  const u32 vo8 = (u32)(wave * 32 + g * 8) * 8192 + c * 16;
  const u32 vok = (u32)wave * 1024 + lane * 16;
#define ISSUE(n)                                                                                              \
  do {                                                                                                        \
    SB();                                                                                                     \
    _Pragma("unroll") for (int j_ = 0; j_ < 4; ++j_) {                                                        \
      if ((n) < 2) wq[(n) * 4 + j_] = ld_nt(rs_wq, vo8, ((n) * 4 + j_) * 8192);                               \
      else if ((n) < 10) kb[((n) - 2) * 4 + j_] = ld_nt(rs_k, vok, (((n) - 2) * 4 + j_) * 4096);              \
      else if ((n) < 18) vb[((n) - 10) / 2][(((n) - 10) & 1) * 4 + j_] = ld_nt(rs_v, vok, (((n) - 10) * 4 + j_) * 4096); \
      else if ((n) < 20) fa[((n) - 18) * 4 + j_] = ld_nt(rs_wf, vo7, (((n) - 18) * 4 + j_) * 7168);           \
      else if ((n) < 22) fb[((n) - 20) * 4 + j_] = ld_nt(rs_wf, vo7, (128 + ((n) - 20) * 4 + j_) * 7168);     \
      else if ((n) < 24) wo[((n) - 22) * 4 + j_] = ld_nt(rs_wo, vo2o, (j_ * 8 + ((n) - 22)) * 2048);          \
      else wd[((n) - 24) * 4 + j_] = ld_nt(rs_wd, vo2, (((n) - 24) * 4 + j_) * 2048);                         \
    }                                                                                                         \
    SB();                                                                                                     \
  } while (0)
#define ISSUE_A(n)                                                                                            \
  do {                                                                                                        \
    SB();                                                                                                     \
    _Pragma("unroll") for (int j_ = 0; j_ < 4; ++j_) {                                                        \
      if ((n) < 2) wq[(n) * 4 + j_] = ld_nt(rs_wq, vo8, ((n) * 4 + j_) * 8192);                               \
      else kb[((n) - 2) * 4 + j_] = ld_nt(rs_k, vok, (((n) - 2) * 4 + j_) * 4096);                            \
    }                                                                                                         \
    SB();                                                                                                     \
  } while (0)
#define WAITG(n)                                                                                              \
  do {                                                                                                        \
    if ((n) < 2) KEEP(wq[(n) * 4 + 3]);                                                                       \
    else if ((n) < 10) KEEP(kb[((n) - 2) * 4 + 3]);                                                           \
    else if ((n) < 18) KEEP(vb[((n) - 10) / 2][(((n) - 10) & 1) * 4 + 3]);                                    \
    else if ((n) < 20) KEEP(fa[((n) - 18) * 4 + 3]);                                                          \
    else if ((n) < 22) KEEP(fb[((n) - 20) * 4 + 3]);                                                          \
    else if ((n) < 24) KEEP(wo[((n) - 22) * 4 + 3]);                                                          \
    else KEEP(wd[((n) - 24) * 4 + 3]);                                                                        \
  } while (0)
  constexpr int NXT[25] = {2, 3, 4, 4, 5, 6, 7, 8, 9, 10, 11, 13, 14, 15, 15, 16, 17, 18, 19, 20, 21, 22, 23, 24, 25};
#define UPTO(n)                                              \
  do {                                                       \
    if (NXT[n] + 1 <= NXT[(n) + 1]) ISSUE(NXT[n] + 1);       \
    if (NXT[n] + 2 <= NXT[(n) + 1]) ISSUE(NXT[n] + 2);       \
    if (NXT[n] + 3 <= NXT[(n) + 1]) ISSUE(NXT[n] + 3);       \
  } while (0)
  ISSUE_A(0); ISSUE_A(1); ISSUE_A(2);

  const int rgo = xcd * 4 + (r >> 3), cgo = r & 7;
  const rsrc_t rs_v = mk(p.v + ((size_t)pair * SK + split * 512) * 256, 512 * 256);
  const rsrc_t rs_wo = mk(p.wo + (size_t)rgo * 128 * 2048 + cgo * 256, 128 * 2048 - cgo * 256);
  rsrc_t rs_wf = rs_x, rs_wd = rs_x;
  const u32 vo7 = (u32)(wave * 32 + g * 8) * 7168 + c * 16;
  const u32 vo2 = (u32)(wave * 32 + g * 8) * 2048 + c * 16;
  const u32 vo2o = (u32)(wave * 32 + g * 2) * 2048 + c * 16;
  const int shard = ((r >> 2) + r + xcd) & (NSH - 1);

  {
    float v[16];
#pragma unroll
    for (int i = 0; i < 4; ++i) { v[2 * i] = bf_lo(x0[i]); v[2 * i + 1] = bf_hi(x0[i]); v[8 + 2 * i] = bf_lo(x1[i]); v[8 + 2 * i + 1] = bf_hi(x1[i]); }
    float s = 0.f;
#pragma unroll
    for (int i = 0; i < 16; ++i) s += v[i];
    const float mean = wave_sum(s) * (1.f / DM);
    float q = 0.f;
#pragma unroll
    for (int i = 0; i < 16; ++i) { const float d = v[i] - mean; q = fmaf(d, d, q); }
    const float var = wave_sum(q) * (1.f / DM);
    const float mb = rbf(mean), vb_ = rbf(var);
    const float den = rbf(sqrtf(rbf(vb_ + 1e-5f)));
    const float rd = frcp(den);
    WAITG(0); UPTO(0);
    u32x4 o0, o1;
#pragma unroll
    for (int i = 0; i < 8; ++i) {
      const float a0 = rbf(v[2 * i] - mb), a1 = rbf(v[2 * i + 1] - mb);
      float q0 = a0 * rd, q1 = a1 * rd;
      q0 = fmaf(fmaf(-q0, den, a0), rd, q0); q1 = fmaf(fmaf(-q1, den, a1), rd, q1);
      if (i < 4) o0[i] = cvt_pk(q0, q1); else o1[i - 4] = cvt_pk(q0, q1);
    }
    asm volatile("s_waitcnt lgkmcnt(0)" ::"s"(tw0), "s"(tw1), "s"(tw2), "s"(tw3), "s"(tw4));
    char* d = lds + L_XN + wave * 2064 + lane * 32;
    *(u32x4*)d = o0; *(u32x4*)(d + 16) = o1;
  }
  wg_barrier();
  const u32x4 xq = *(const u32x4*)(lds + L_XN + (c & 3) * 2064 + xcd * 256 + wave * 64 + g * 16);
  const u64 epoch = (((u64)(u32)__builtin_amdgcn_readfirstlane(e2[1])) << 32 | (u32)__builtin_amdgcn_readfirstlane(e2[0])) + 1ull;
  const u32 T12 = (u32)epoch & 0xfffu;
  const u32 QTAG = ((u32)epoch & 0xfffffu) << 12;
  __builtin_amdgcn_raw_buffer_store_b32(QTAG, rs_ws, (bid == 0 && tid < NSH) ? WS_CF + tid * 256 : OOB, 0, 16);

  WAITG(1); UPTO(1);
  {
    f32x4 acc[8];
#pragma unroll
    for (int i = 0; i < 8; ++i) acc[i] = mfma(xq, colfrag(wq, i), f32x4{0.f, 0.f, 0.f, 0.f});
    if (g == 0) {
      char* d = lds + L_REDQ + wave * 2048;
#pragma unroll
      for (int b = 0; b < 4; ++b) {
        *(f32x4*)(d + b * 512 + ((2 * c) ^ (c >> 2 & 1)) * 16) = f32x4{acc[0][b], acc[1][b], acc[2][b], acc[3][b]};
        *(f32x4*)(d + b * 512 + ((2 * c + 1) ^ (c >> 2 & 1)) * 16) = f32x4{acc[4][b], acc[5][b], acc[6][b], acc[7][b]};
      }
    }
  }
  wg_barrier();
  {
    const int t7 = tid & 127;
    const char* s = lds + L_REDQ + (t7 ^ (t7 >> 3 & 1)) * 16;
    const f32x4 a = *(const f32x4*)s + *(const f32x4*)(s + 2048) + *(const f32x4*)(s + 4096) + *(const f32x4*)(s + 6144);
    const u32x4 au = __builtin_bit_cast(u32x4, a);
    const u32 xr = half_add(csum4(au));
    st_sc(au, rs_ws, tid < 128 ? WS_QP + (u32)((xcd * 4 + (t7 >> 5)) * 4096 + r * 128 + (t7 & 31) * 4) * 4 : OOB, 0);
    st_sc(u32x4{(u32)epoch, (u32)(epoch >> 32), xr, 0u}, rs_ws, (tid < 128 && (tid & 31) == 0) ? WS_QC + (u32)(((t7 >> 5) * 32 + r) * 8 + xcd) * 16 : OOB, 0);
  }

  char* kl = lds + L_K + wave * 32768;
#define KSTORE(n)                                                                                   \
  _Pragma("unroll") for (int j = 0; j < 4; ++j) {                                                   \
    const int t_ = ((n) - 2) * 4 + j;                                                               \
    *(u32x4*)(kl + (t_ * 4 + g) * 256 + ((c ^ (g * 4 + j)) * 16)) = kb[t_];                         \
  }
  u32x4 qp[8], qc;
  const u32 qp_off = WS_QP + (u32)(b_att * 4096 + kvh * 1024 + tid * 4) * 4;
  const u32 qc_off = WS_QC + (u32)((b_att * 32 + kvh * 8 + (tid >> 5)) * 8 + (tid & 7)) * 16;
  WAITG(2); UPTO(2); KSTORE(2)
  WAITG(3); UPTO(3); KSTORE(3)
  WAITG(4); UPTO(4); KSTORE(4)
  WAITG(5); UPTO(5); KSTORE(5)
  WAITG(6); SB(); qp[0] = ld_sc(rs_ws, qp_off, 0); qp[1] = ld_sc(rs_ws, qp_off, 65536); UPTO(6); KSTORE(6)
  WAITG(7); SB(); qp[2] = ld_sc(rs_ws, qp_off, 2 * 65536); qp[3] = ld_sc(rs_ws, qp_off, 3 * 65536); UPTO(7); KSTORE(7)
  WAITG(8); SB(); qp[4] = ld_sc(rs_ws, qp_off, 4 * 65536); qp[5] = ld_sc(rs_ws, qp_off, 5 * 65536); UPTO(8); KSTORE(8)
  WAITG(9); SB(); qp[6] = ld_sc(rs_ws, qp_off, 6 * 65536); qp[7] = ld_sc(rs_ws, qp_off, 7 * 65536); qc = ld_sc(rs_ws, qc_off, 0); UPTO(9); KSTORE(9)
  WAITG(10); UPTO(10);
#define QBAD(bad)                                                                                   \
  {                                                                                                 \
    u32 x_ = (tid & 31) < 8 ? 0u - qc[2] : 0u;                                                      \
    _Pragma("unroll") for (int s = 0; s < 8; ++s) x_ += csum4(qp[s]);                               \
    bad = half_add(x_) | (qc[0] ^ (u32)epoch) | (qc[1] ^ (u32)(epoch >> 32));                       \
  }
  {
    u32 bad;
    QBAD(bad)
    if (!__all(bad == 0)) {
      for (int spins = 0; spins < SPIN_CAP; ++spins) {
        asm volatile("" ::: "memory");
#pragma unroll
        for (int s = 0; s < 8; ++s) qp[s] = ld_sc(rs_ws, qp_off, s * 65536);
        qc = ld_sc(rs_ws, qc_off, 0);
        QBAD(bad)
        if (__all(bad == 0)) break;
      }
    }
    f32x4 a = __builtin_bit_cast(f32x4, qp[0]);
#pragma unroll
    for (int s = 1; s < 8; ++s) a += __builtin_bit_cast(f32x4, qp[s]);
    *(u32x2*)(lds + L_QS + (tid >> 5) * 272 + (tid & 31) * 8) = u32x2{cvt_pk(a[0], a[1]), cvt_pk(a[2], a[3])};
  }
  wg_barrier();

  u32x4 pf[2];
  float m_w, l_w;
  {
    u32x4 qlo[4], qhi[4];
#pragma unroll
    for (int s = 0; s < 4; ++s) {
      const u32x4 qv = *(const u32x4*)(lds + L_QS + (c & 7) * 272 + (4 * s + g) * 16);
      const u32x4 z = {0u, 0u, 0u, 0u};
      qlo[s] = c < 8 ? qv : z; qhi[s] = c < 8 ? z : qv;
    }
    const int rho = (c & 3) * 4 + (c >> 2);
    float sc[4][4];
#pragma unroll
    for (int u = 0; u < 2; ++u) {
#pragma unroll
      for (int a = 0; a < 2; ++a) {
        f32x4 acc = {0.f, 0.f, 0.f, 0.f};
#pragma unroll
        for (int s = 0; s < 4; ++s) {
          const u32x4 kf = *(const u32x4*)(kl + ((u * 4 + a) * 16 + rho) * 256 + (((4 * s + g) ^ c) * 16));
          acc = mfma(kf, qlo[s], acc);
        }
#pragma unroll
        for (int s = 0; s < 4; ++s) {
          const u32x4 kf = *(const u32x4*)(kl + ((u * 4 + 2 + a) * 16 + rho) * 256 + (((4 * s + g) ^ c) * 16));
          acc = mfma(kf, qhi[s], acc);
        }
        const u32 p01 = cvt_pk(acc[0], acc[1]), p23 = cvt_pk(acc[2], acc[3]);
        const u32 r01 = cvt_pk(bf_lo(p01) * 0.08838834764831845f, bf_hi(p01) * 0.08838834764831845f);
        const u32 r23 = cvt_pk(bf_lo(p23) * 0.08838834764831845f, bf_hi(p23) * 0.08838834764831845f);
        sc[u * 2 + a][0] = bf_lo(r01); sc[u * 2 + a][1] = bf_hi(r01); sc[u * 2 + a][2] = bf_lo(r23); sc[u * 2 + a][3] = bf_hi(r23);
      }
      if (u == 0) { WAITG(11); UPTO(11); }
    }
    float m = sc[0][0];
#pragma unroll
    for (int a = 0; a < 4; ++a)
#pragma unroll
      for (int i = 0; i < 4; ++i) m = fmaxf(m, sc[a][i]);
    m = fmaxf(m, dpp<XOR8>(m));
    m = xrow_max(m);
    const float ml2 = m * 1.4426950408889634f;
    float l = 0.f;
#pragma unroll
    for (int a = 0; a < 4; ++a) {
#pragma unroll
      for (int i = 0; i < 4; ++i) { sc[a][i] = __builtin_amdgcn_exp2f(fmaf(sc[a][i], 1.4426950408889634f, -ml2)); l += sc[a][i]; }
    }
#pragma unroll
    for (int u = 0; u < 2; ++u) {
      pf[u][0] = cvt_pk(sc[2 * u][0], sc[2 * u][1]);
      pf[u][1] = cvt_pk(sc[2 * u][2], sc[2 * u][3]);
      pf[u][2] = cvt_pk(sc[2 * u + 1][0], sc[2 * u + 1][1]);
      pf[u][3] = cvt_pk(sc[2 * u + 1][2], sc[2 * u + 1][3]);
    }
    l += dpp<XOR8>(l);
    m_w = m; l_w = xrow_sum(l);
  }

  f32x4 oa[8];
  u32x4 pl[2], ph[2];
  {
    const u32x4 z = {0u, 0u, 0u, 0u};
#pragma unroll
    for (int u = 0; u < 2; ++u) { pl[u] = c < 8 ? pf[u] : z; ph[u] = c < 8 ? z : pf[u]; }
  }
  WAITG(12); UPTO(12);
#pragma unroll
  for (int i = 0; i < 8; ++i) oa[i] = mfma(colfrag(vb[0], i), pl[0], f32x4{0.f, 0.f, 0.f, 0.f});
  WAITG(13); UPTO(13);
#pragma unroll
  for (int i = 0; i < 8; ++i) oa[i] = mfma(colfrag(vb[1], i), ph[0], oa[i]);
  WAITG(14); SB();
  u32 clm = at_add(1u, rs_ws, tid == 0 ? WS_CF + shard * 256 : OOB);
  UPTO(14);
  WAITG(15); UPTO(15);
#pragma unroll
  for (int i = 0; i < 8; ++i) oa[i] = mfma(colfrag(vb[2], i), pl[1], oa[i]);
  WAITG(16);
  if (tid == 0 && (clm & 0xfffff000u) != QTAG) {
    for (int spins = 0; spins < SPIN_CAP; ++spins) {
      asm volatile("" ::: "memory");
      if ((__builtin_amdgcn_raw_buffer_load_b32(rs_ws, WS_CF + shard * 256, 0, 16) & 0xfffff000u) == QTAG) break;
    }
    clm = at_add(1u, rs_ws, WS_CF + shard * 256);
  }
  if (tid == 0) *(u32*)(lds + L_CL) = min(clm & 0xfffu, (u32)(256 / NSH - 1));
  wg_barrier();
  const int kq = __builtin_amdgcn_readfirstlane(*(const u32*)(lds + L_CL));
  constexpr int PF = 224 / NSH, PS = 32 / NSH;
  const bool has_f = kq < PF, has_d = kq < PF - PS || kq >= PF;
  const int tfc = has_f ? shard * PF + kq : 0;
  const int kgf = tfc / 28, cgf = tfc - kgf * 28;
  rs_wf = mk(((kgf & 1) ? p.wu : p.wg) + (size_t)(256 * (kgf >> 1)) * 7168 + cgf * 256, has_f ? 256 * 7168 - cgf * 256 : 0);
  const int tdn = shard * PF + (kq >= PF ? kq - PF : (has_d ? kq + PS : 0));
  const int rgd = tdn >> 3, cgd = tdn & 7;
  rs_wd = mk(p.wd + (size_t)rgd * 128 * 2048 + cgd * 256, has_d ? 128 * 2048 - cgd * 256 : 0);
  const rsrc_t rs_wsd = mk(p.ws, has_d ? WS_END : 0u);
  UPTO(16);
  WAITG(17); UPTO(17);
#pragma unroll
  for (int i = 0; i < 8; ++i) oa[i] = mfma(colfrag(vb[3], i), ph[1], oa[i]);
  {
    char* d = kl + LK_MRG + c * 512 + g * 128;
#pragma unroll
    for (int i = 0; i < 8; ++i) *(f32x4*)(d + (i ^ (c & 7)) * 16) = oa[i];
    if (lane < 8) *(float2*)(lds + L_ML + (wave * 8 + c) * 8) = make_float2(m_w, l_w);
  }
  wg_barrier();
  {
    const int hh = tid >> 5, d4 = tid & 31;
    float2 ml[4];
#pragma unroll
    for (int w = 0; w < 4; ++w) ml[w] = *(const float2*)(lds + L_ML + (w * 8 + hh) * 8);
    const float M = fmaxf(fmaxf(ml[0].x, ml[1].x), fmaxf(ml[2].x, ml[3].x));
    f32x4 o = {0.f, 0.f, 0.f, 0.f};
    float L = 0.f;
#pragma unroll
    for (int w = 0; w < 4; ++w) {
      const float f = fexp(ml[w].x - M);
      L = fmaf(f, ml[w].y, L);
      const char* s = lds + L_K + w * 32768 + LK_MRG + hh * 512 + (d4 ^ hh) * 16;
      o += f * (*(const f32x4*)s + *(const f32x4*)(s + 4096));
    }
    o *= frcp(L);
    const u32 p0 = cvt_pk(o[0], o[1]), p1 = cvt_pk(o[2], o[3]);
    const u32x4 pk = {p0, p1, dppu<XOR1>(p0), dppu<XOR1>(p1)};
    const u32 xr = half_add(p0 + p1 + ((d4 & 1) ? p1 : 0u)) ^ __float_as_uint(M) ^ __float_as_uint(L) ^ (u32)(epoch >> 32);
    const u32 slot = (u32)((b_att * 32 + kvh * 8 + hh) * 16 + split);
    st_sc(pk, rs_ws, (d4 & 1) == 0 ? WS_OP + slot * 256 + d4 * 8 : OOB, 0);
    st_sc(u32x4{__float_as_uint(M), __float_as_uint(L), xr, (u32)epoch}, rs_ws, d4 == 0 ? WS_ML + slot * 16 : OOB, 0);
  }

  u32x4 og[4], mlv;
  const u32 og_off = WS_OP + (u32)((wave * 32 + rgo) * 16 + g * 4) * 256 + c * 16;
  const u32 ml_off = WS_ML + (u32)((wave * 32 + rgo) * 16 + ((c + g * 4) & 15)) * 16;
  {
    f32x4 acc[2] = {{0.f, 0.f, 0.f, 0.f}, {0.f, 0.f, 0.f, 0.f}};
    const u32x4 xfa = *(const u32x4*)(lds + L_XN + (c & 3) * 2064 + (kgf >> 1) * 512 + wave * 64 + g * 16);
    const u32x4 xfb = *(const u32x4*)(lds + L_XN + (c & 3) * 2064 + (kgf >> 1) * 512 + 256 + wave * 64 + g * 16);
    u32x4 ama[4], amb[4];
    amask(xfa, c, ama); amask(xfb, c, amb);
    WAITG(18); UPTO(18);
    WAITG(19); UPTO(19);
#pragma unroll
    for (int i = 0; i < 8; ++i) acc[i >> 2] = mfma(ama[i & 3], colfrag(fa, i), acc[i >> 2]);
    WAITG(20); UPTO(20);
    WAITG(21); SB();
#pragma unroll
    for (int i = 0; i < 8; ++i) acc[i >> 2] = mfma(amb[i & 3], colfrag(fb, i), acc[i >> 2]);
    tile_store2(kl + LK_RF, acc, c, g);
  }
  wg_barrier();
  {
    const char* s = lds + L_K + LK_RF + tile_off(tid & 127);
    f32x4 a = (*(const f32x4*)s + *(const f32x4*)(s + 32768)) + (*(const f32x4*)(s + 65536) + *(const f32x4*)(s + 98304));
    st_sc(tag4(a, T12), rs_ws, (has_f && tid < 128) ? WS_GU + (u32)(kgf * DF + cgf * 128 + tid) * 16 : OOB, 0);
  }
  SB();
#pragma unroll
  for (int s = 0; s < 4; ++s) og[s] = ld_sc(rs_ws, og_off, s * 256);
  mlv = ld_sc(rs_ws, ml_off, 0);
  UPTO(21);
  SB();
  WAITG(22); UPTO(22);
#define OBAD(bad)                                                                                                  \
  {                                                                                                                \
    const u32 want = mlv[2] ^ mlv[0] ^ mlv[1] ^ (u32)(epoch >> 32);              \
    u32 x_ = c < 4 ? 0u - want : 0u;                                    \
    _Pragma("unroll") for (int s = 0; s < 4; ++s) x_ += csum4(og[s]);                                              \
    bad = row_add(x_) | (mlv[3] ^ (u32)epoch);                                                                     \
  }
  {
    u32 bad;
    OBAD(bad)
    if (!__all(bad == 0)) {
      for (int spins = 0; spins < SPIN_CAP; ++spins) {
        asm volatile("" ::: "memory");
#pragma unroll
        for (int s = 0; s < 4; ++s) og[s] = ld_sc(rs_ws, og_off, s * 256);
        mlv = ld_sc(rs_ws, ml_off, 0);
        OBAD(bad)
        if (__all(bad == 0)) break;
      }
    }
  }
  {
    const float ms = __uint_as_float(mlv[0]), ls = __uint_as_float(mlv[1]);
    const float M = row16_max(ms);
    const float wl = fexp(ms - M) * ls;
    const float w = wl * frcp(row16_sum(wl));
    const float w0 = dpp<0x150>(w), w1 = dpp<0x151>(w), w2 = dpp<0x152>(w), w3 = dpp<0x153>(w);
    u32x4 pk;
#pragma unroll
    for (int i = 0; i < 4; ++i) {
      float lo = w0 * bf_lo(og[0][i]), hi = w0 * bf_hi(og[0][i]);
      lo = fmaf(w1, bf_lo(og[1][i]), lo); hi = fmaf(w1, bf_hi(og[1][i]), hi);
      lo = fmaf(w2, bf_lo(og[2][i]), lo); hi = fmaf(w2, bf_hi(og[2][i]), hi);
      lo = fmaf(w3, bf_lo(og[3][i]), lo); hi = fmaf(w3, bf_hi(og[3][i]), hi);
      pk[i] = cvt_pk(xrow_sum(lo), xrow_sum(hi));
    }
    if (lane < 16) *(u32x4*)(lds + L_OS + wave * 256 + lane * 16) = pk;
  }
  WAITG(23);
  u32x4 gp[4];
  const u32 gu_off = WS_GU + (u32)((lane >> 5) * DF + rgd * 128 + wave * 32 + (lane & 31)) * 16;
  asm volatile("" ::: "memory");
#pragma unroll
  for (int s = 0; s < 4; ++s) gp[s] = ld_sc(rs_wsd, gu_off, s * (2 * DF * 16));
  SB();
  UPTO(23);
  u32x4 of_[8];
#pragma unroll
  for (int i = 0; i < 8; ++i) of_[i] = colfrag(wo, i);
  wg_barrier();
  {
    const u32x4 of = *(const u32x4*)(lds + L_OS + (c & 3) * 256 + wave * 64 + g * 16);
    f32x4 acc[2] = {{0.f, 0.f, 0.f, 0.f}, {0.f, 0.f, 0.f, 0.f}};
    u32x4 am[4];
    amask(of, c, am);
#pragma unroll
    for (int i = 0; i < 8; ++i) acc[i & 1] = mfma(am[i >> 1], of_[(i & 1) * 4 + (i >> 1)], acc[i & 1]);
    tile_store2(kl + LK_RO, acc, c, g);
  }
  wg_barrier();
  {
    const char* s = lds + L_K + LK_RO + tile_off(tid & 127);
    f32x4 a = (*(const f32x4*)s + *(const f32x4*)(s + 32768)) + (*(const f32x4*)(s + 65536) + *(const f32x4*)(s + 98304));
    st_sc(tag4(a, T12), rs_ws, tid < 128 ? WS_FP + (u32)(rgo * 1024 + cgo * 128 + tid) * 16 : OOB, 0);
  }
  SB();
  {
    u32 bad = 0;
#pragma unroll
    for (int s = 0; s < 4; ++s) bad |= tagbad(gp[s], T12);
    if (has_d && !__all(bad == 0)) {
      for (int spins = 0; spins < SPIN_CAP; ++spins) {
        asm volatile("" ::: "memory");
#pragma unroll
        for (int s = 0; s < 4; ++s) gp[s] = ld_sc(rs_wsd, gu_off, s * (2 * DF * 16));
        bad = 0;
#pragma unroll
        for (int s = 0; s < 4; ++s) bad |= tagbad(gp[s], T12);
        if (__all(bad == 0)) break;
      }
    }
    const f32x4 a = (untag(gp[0]) + untag(gp[1])) + (untag(gp[2]) + untag(gp[3]));
#pragma unroll
    for (int b = 0; b < 4; ++b) {
      const float v = rbf(a[b]);
      auto t = __builtin_amdgcn_permlane32_swap(__float_as_uint(v), __float_as_uint(v), false, false);
      const float gv = __uint_as_float(t[0]), uv = __uint_as_float(t[1]);
      const float inner = 0.7978845608028654f * (gv + 0.044715f * gv * gv * gv);
      const float th = 1.f - 2.f * frcp(fexp(2.f * inner) + 1.f);
      const float ge = rbf(0.5f * gv * (1.f + th));
      const u32 hv = cvt_pk(ge * uv, 0.f);
      if (lane < 32) *(unsigned short*)(kl + LK_HS + b * 64 + lane * 2) = (unsigned short)hv;
    }
  }
  asm volatile("s_waitcnt lgkmcnt(0)" ::: "memory");
  SB();
  u32x4 df[8];
#pragma unroll
  for (int i = 0; i < 8; ++i) df[i] = colfrag(wd, i);
  {
    const u32x4 hf = *(const u32x4*)(kl + LK_HS + (c & 3) * 64 + g * 16);
    f32x4 acc[2] = {{0.f, 0.f, 0.f, 0.f}, {0.f, 0.f, 0.f, 0.f}};
    u32x4 am[4];
    amask(hf, c, am);
#pragma unroll
    for (int i = 0; i < 8; ++i) acc[i & 1] = mfma(am[i >> 1], df[(i & 1) * 4 + (i >> 1)], acc[i & 1]);
    tile_store2(kl + LK_RD, acc, c, g);
  }
  wg_barrier();
  {
    const char* s = lds + L_K + LK_RD + tile_off(tid & 127);
    f32x4 a = (*(const f32x4*)s + *(const f32x4*)(s + 32768)) + (*(const f32x4*)(s + 65536) + *(const f32x4*)(s + 98304));
    st_sc(tag4(a, T12), rs_wsd, tid < 128 ? WS_FP + (u32)((32 + rgd) * 1024 + cgd * 128 + tid) * 16 : OOB, 0);
  }
  SB();
  {
    const int col0 = (xcd * 32 + r) * 4;
    const int ks = tid >> 2, cc = tid & 3;
    const bool act = ks < 60;
    const u32 off = act ? WS_FP + (u32)(ks * 1024 + col0 + cc) * 16 : OOB;
    const unsigned short xin = *(const unsigned short*)(p.x + (((tid >> 2) & 3) * DM + col0 + cc) * 2);
    u32x4 fv;
    for (int spins = 0; spins < SPIN_CAP; ++spins) {
      fv = ld_sc(rs_ws, off, 0);
      const u32 bad = act ? tagbad(fv, T12) : 0u;
      if (__all(bad == 0)) break;
      asm volatile("" ::: "memory");
    }
    f32x4 a = untag(fv);
#pragma unroll
    for (int i = 0; i < 4; ++i) {
      float t = a[i];
      t += dpp<0x124>(t);
      t += dpp<XOR8>(t);
      a[i] = xrow_sum(t);
    }
    if (lane < 4) *(f32x4*)(lds + L_K + LK_FIN + (wave * 4 + lane) * 16) = a;
    wg_barrier();
    if (tid < 16) {
      const int b = tid >> 2, cc2 = tid & 3;
      const float* fs = (const float*)(lds + L_K + LK_FIN) + cc2 * 4 + b;
      const float s = rbf(fs[0] + fs[16]);
      const float dd = rbf(fs[32] + fs[48]);
      const float gt = rbf(dd * frcp(1.f + fexp(-dd)));
      const float o = bf_lo(xin) + rbf(s * gt);
      *(unsigned short*)(p.out + (b * DM + col0 + cc2) * 2) = (unsigned short)cvt_pk(o, o);
    }
    if (bid == 0 && tid == 0) st_sc64(u32x2{(u32)epoch, (u32)(epoch >> 32)}, rs_ws, WS_E, 0);
  }
  KEEP(cw0);
  asm volatile(".Lfdk_end:");
}

extern "C" void kernel_launch(void* const* d_in, const int* in_sizes, int n_in, void* d_out, int out_size, void* d_ws,
                              size_t ws_size, hipStream_t stream) {
  Params p;
  p.x = (const char*)d_in[0]; p.k = (const char*)d_in[1]; p.v = (const char*)d_in[2];
  p.wq = (const char*)d_in[3]; p.wo = (const char*)d_in[4];
  p.wg = (const char*)d_in[6]; p.wu = (const char*)d_in[7]; p.wd = (const char*)d_in[8];
  p.out = (char*)d_out; p.ws = (char*)d_ws;
  (void)hipFuncSetAttribute((const void*)fused_decode_kernel, hipFuncAttributeMaxDynamicSharedMemorySize, L_END);
  fused_decode_kernel<<<NWG, NTHR, L_END, stream>>>(p);
}
```
